```python
import torch

def _softmax(x, axis=-1):
    return torch.softmax(x, dim=axis)
def _gelu(x):
    return torch.nn.functional.gelu(x, approximate="tanh")
def _silu(x):
    return torch.nn.functional.silu(x)
def _sigmoid(x):
    return torch.sigmoid(x)
def _layernorm(x, axis=-1, eps=1e-5):
    m = x.mean(dim=axis, keepdim=True)
    v = x.var(dim=axis, keepdim=True, unbiased=False)
    return (x - m) / torch.sqrt(v + eps)
def _scatter_add(tgt, idx, src, axis=0):
    return tgt.clone().index_add_(axis, idx, src)
def _segment_sum(src, seg, n):
    out = torch.zeros((n,) + src.shape[1:], dtype=src.dtype)
    return out.index_add_(0, seg, src)
def _pad(x, pw):
    flat = tuple(v for pair in reversed(pw) for v in pair)
    return torch.nn.functional.pad(x, flat)

kernel_name = 'standalone__mha_block__prompt'

def setup_inputs():

    _w = (lambda a: (lambda t: t.to(torch.bfloat16) if t.is_floating_point() else t)(torch.as_tensor(a)))
    B, S, D, H = 1, 2048, 640, 32
    return {
        "x": _w((lambda *s: torch.randn(*s, dtype=torch.bfloat16) * 0.2)(B, S, D)),
        "wqkv": _w((lambda *s: torch.randn(*s, dtype=torch.bfloat16) * 0.2)(D, 3 * D)),
        "wo": _w((lambda *s: torch.randn(*s, dtype=torch.bfloat16) * 0.05)(D, D)),
    }

def reference(x, wqkv, wo):
    B, S, D = x.shape
    H = 32
    Dh = D // H
    qkv = (lambda x, *d: x.permute(*d))((x @ wqkv).reshape(B, S, 3, H, Dh), 2, 0, 3, 1, 4)
    q, k, v = qkv[0], qkv[1], qkv[2]
    scores = torch.einsum("bhqd,bhkd->bhqk", q, k) * (Dh ** -0.5)
    p = _softmax(scores, axis=-1)
    o = torch.einsum("bhqk,bhkd->bhqd", p, v)
    return (lambda x, *d: x.permute(*d))(o, 0, 2, 1, 3).reshape(B, S, D) @ wo
```

```cpp
#include <hip/hip_runtime.h>
#include <hip/hip_bf16.h>
#include <stdint.h>


typedef unsigned short u16;
typedef __attribute__((ext_vector_type(8))) short bf16x8;
typedef __attribute__((ext_vector_type(4))) short s16x4;
typedef __attribute__((ext_vector_type(16))) float f32x16;
typedef __attribute__((ext_vector_type(4))) float f32x4;
typedef __attribute__((ext_vector_type(4))) unsigned u32x4;

constexpr int S_ = 2048, D_ = 640, H_ = 32, DH = 20, N3 = 1920;
constexpr int BK = 64;

__device__ __forceinline__ unsigned cvtpk(float lo, float hi) {
  unsigned r; asm("v_cvt_pk_bf16_f32 %0, %1, %2" : "=v"(r) : "v"(lo), "v"(hi)); return r;
}
__device__ __forceinline__ uint2 pack4(float a, float b, float c, float d) {
  uint2 r; r.x = cvtpk(a, b); r.y = cvtpk(c, d); return r;
}
__device__ __forceinline__ s16x4 tr_read(const u16* p) {
  typedef __attribute__((address_space(3))) s16x4* lp;
  return __builtin_amdgcn_ds_read_tr16_b64_v4i16((lp)p);
}

template <int BM, int BN>
struct GemmSmem {
  u16 As[BM][BK + 8];
  u16 Bs[BK][BN + 32];
};

template <int BM, int BN, int TM, int TN>
__device__ __forceinline__ void gemm_mainloop(const u16* __restrict__ A, int lda, const u16* __restrict__ B, int ldb,
                                              int m0, int n0, int K, GemmSmem<BM, BN>& sm, f32x16 (&acc)[TM][TN], bool swap) {
  const int tid = threadIdx.x, lane = tid & 63, wid = tid >> 6;
  const int wm = wid >> 1, wn = wid & 1;
  const int r32 = lane & 31, hf = lane >> 5;
  constexpr int NA = BM * BK / 8 / 256, NB = BK * BN / 8 / 256;
  u32x4 ra[NA], rb[NB];
#define GEMM_GLOAD(kt)                                                                         \
  _Pragma("unroll") for (int i = 0; i < NA; ++i) {                                             \
    int c = tid + i * 256; int row = c >> 3, col = (c & 7) * 8;                                \
    ra[i] = *reinterpret_cast<const u32x4*>(A + (size_t)(m0 + row) * lda + (kt) * BK + col);   \
  }                                                                                            \
  _Pragma("unroll") for (int i = 0; i < NB; ++i) {                                             \
    int c = tid + i * 256; int row = c / (BN / 8), col = (c % (BN / 8)) * 8;                   \
    rb[i] = *reinterpret_cast<const u32x4*>(B + (size_t)((kt) * BK + row) * ldb + n0 + col);   \
  }
#define GEMM_SSTORE()                                                                          \
  _Pragma("unroll") for (int i = 0; i < NA; ++i) {                                             \
    int c = tid + i * 256; int row = c >> 3, col = (c & 7) * 8;                                \
    *reinterpret_cast<u32x4*>(&sm.As[row][col]) = ra[i];                                       \
  }                                                                                            \
  _Pragma("unroll") for (int i = 0; i < NB; ++i) {                                             \
    int c = tid + i * 256; int row = c / (BN / 8), col = (c % (BN / 8)) * 8;                   \
    *reinterpret_cast<u32x4*>(&sm.Bs[row][col]) = rb[i];                                       \
  }
#pragma unroll
  for (int i = 0; i < TM; ++i)
#pragma unroll
    for (int j = 0; j < TN; ++j) acc[i][j] = f32x16{};
  const int KT = K / BK;
  GEMM_GLOAD(0);
  for (int kt = 0; kt < KT; ++kt) {
    __syncthreads();
    GEMM_SSTORE();
    __syncthreads();
    if (kt + 1 < KT) { GEMM_GLOAD(kt + 1); }
#pragma unroll
    for (int ks = 0; ks < BK / 16; ++ks) {
      bf16x8 af[TM], bfr[TN];
#pragma unroll
      for (int i = 0; i < TM; ++i)
        af[i] = *reinterpret_cast<const bf16x8*>(&sm.As[wm * TM * 32 + i * 32 + r32][ks * 16 + hf * 8]);
#pragma unroll
      for (int j = 0; j < TN; ++j) {
        const int t = lane & 15, g = (lane >> 4) & 1;
        const u16* p = &sm.Bs[ks * 16 + hf * 8 + (t >> 2)][wn * TN * 32 + j * 32 + g * 16 + (t & 3) * 4];
        s16x4 v0 = tr_read(p), v1 = tr_read(p + 4 * (BN + 32));
        bfr[j] = bf16x8{v0[0], v0[1], v0[2], v0[3], v1[0], v1[1], v1[2], v1[3]};
      }
      if (!swap) {
#pragma unroll
        for (int i = 0; i < TM; ++i)
#pragma unroll
          for (int j = 0; j < TN; ++j) acc[i][j] = __builtin_amdgcn_mfma_f32_32x32x16_bf16(af[i], bfr[j], acc[i][j], 0, 0, 0);
      } else {
#pragma unroll
        for (int i = 0; i < TM; ++i)
#pragma unroll
          for (int j = 0; j < TN; ++j) acc[i][j] = __builtin_amdgcn_mfma_f32_32x32x16_bf16(bfr[j], af[i], acc[i][j], 0, 0, 0);
      }
    }
  }
}

__global__ __launch_bounds__(256) void qkv_kernel(const u16* __restrict__ x, const u16* __restrict__ w,
                                                  u16* __restrict__ Qb, u16* __restrict__ Kb, u16* __restrict__ Vt) {
  constexpr int BM = 128, BN = 128;
  __shared__ GemmSmem<BM, BN> sm;
  const int m0 = blockIdx.y * BM, n0 = blockIdx.x * BN;
  const bool isv = n0 >= 2 * D_;
  f32x16 acc[2][2];
  gemm_mainloop<BM, BN, 2, 2>(x, D_, w, N3, m0, n0, D_, sm, acc, !isv);
  const int tid = threadIdx.x, lane = tid & 63, wid = tid >> 6;
  const int wm = wid >> 1, wn = wid & 1, r32 = lane & 31, hf = lane >> 5;
  const float qscale = 0.22360679774997896f * 1.4426950408889634f;
  if (!isv) {
#pragma unroll
    for (int i = 0; i < 2; ++i)
#pragma unroll
      for (int j = 0; j < 2; ++j) {
        const int s = m0 + wm * 64 + i * 32 + r32;
#pragma unroll
        for (int g = 0; g < 4; ++g) {
          const int n = n0 + wn * 64 + j * 32 + 8 * g + 4 * hf;
          const int which = n >= D_ ? 1 : 0;
          const int nn = n - which * D_;
          const int h = nn / DH, d = nn - h * DH;
          const float sc = which ? 1.f : qscale;
          uint2 v = pack4(acc[i][j][4 * g] * sc, acc[i][j][4 * g + 1] * sc, acc[i][j][4 * g + 2] * sc, acc[i][j][4 * g + 3] * sc);
          u16* dst = (which ? Kb : Qb) + ((size_t)h * S_ + s) * DH + d;
          *reinterpret_cast<uint2*>(dst) = v;
        }
      }
  } else {
#pragma unroll
    for (int i = 0; i < 2; ++i)
#pragma unroll
      for (int j = 0; j < 2; ++j) {
        const int nn = n0 + wn * 64 + j * 32 + r32 - 2 * D_;
        const int h = nn / DH, d = nn - h * DH;
#pragma unroll
        for (int g = 0; g < 4; ++g) {
          const int s = m0 + wm * 64 + i * 32 + 8 * g + 4 * hf;
          const int pos = (s & ~0xC) | ((s & 4) << 1) | ((s & 8) >> 1);
          uint2 v = pack4(acc[i][j][4 * g], acc[i][j][4 * g + 1], acc[i][j][4 * g + 2], acc[i][j][4 * g + 3]);
          *reinterpret_cast<uint2*>(Vt + ((size_t)h * DH + d) * S_ + pos) = v;
        }
      }
  }
}

constexpr int KSTR = 40;
constexpr int VSTR = 72;
constexpr int KVB = 64;

__global__ __launch_bounds__(256) void attn_kernel(const u16* __restrict__ Qb, const u16* __restrict__ Kb,
                                                   const u16* __restrict__ Vt, u16* __restrict__ O) {
  __shared__ u16 Ks[2][KVB][KSTR];
  __shared__ u16 Vs[2][32][VSTR];
  const int tid = threadIdx.x, lane = tid & 63, wid = tid >> 6;
  const int r32 = lane & 31, hf = lane >> 5;
  const int h = blockIdx.y;
  const int q = blockIdx.x * 128 + wid * 32 + r32;
  for (int i = tid; i < 2 * KVB * (KSTR - DH); i += 256) {
    int b = i / (KVB * (KSTR - DH)), rem = i % (KVB * (KSTR - DH));
    Ks[b][rem / (KSTR - DH)][DH + rem % (KSTR - DH)] = 0;
  }
  for (int i = tid; i < 2 * 12 * VSTR; i += 256) {
    int b = i / (12 * VSTR), rem = i % (12 * VSTR);
    int row = DH + rem / VSTR;
    Vs[b][row][rem % VSTR] = (row == DH) ? (u16)0x3F80 : (u16)0;
  }
  bf16x8 qf0, qf1;
  {
    const u16* qp = Qb + ((size_t)h * S_ + q) * DH;
    uint2 a0 = *reinterpret_cast<const uint2*>(qp + hf * 8);
    uint2 a1 = *reinterpret_cast<const uint2*>(qp + hf * 8 + 4);
    uint2 b0 = *reinterpret_cast<const uint2*>(qp + 16);
    uint4 f0 = make_uint4(a0.x, a0.y, a1.x, a1.y);
    uint4 f1 = hf ? make_uint4(0, 0, 0, 0) : make_uint4(b0.x, b0.y, 0, 0);
    qf0 = *reinterpret_cast<bf16x8*>(&f0); qf1 = *reinterpret_cast<bf16x8*>(&f1);
  }
  const u16* Kh = Kb + (size_t)h * S_ * DH;
  const u16* Vh = Vt + (size_t)h * DH * S_;
  uint4 kreg, vreg;
  const bool ldk = tid < 160, ldv = tid >= 96;
  const int vc = tid - 96, vrow = vc >> 3, vcol = (vc & 7) * 8;
  auto gload = [&](int t) {
    if (ldk) kreg = *reinterpret_cast<const uint4*>(Kh + (size_t)t * KVB * DH + tid * 8);
    if (ldv) vreg = *reinterpret_cast<const uint4*>(Vh + (size_t)vrow * S_ + t * KVB + vcol);
  };
  auto sstore = [&](int b) {
    if (ldk) {
      const int u0 = 2 * tid, u1 = 2 * tid + 1;
      *reinterpret_cast<uint2*>(&Ks[b][u0 / 5][(u0 % 5) * 4]) = make_uint2(kreg.x, kreg.y);
      *reinterpret_cast<uint2*>(&Ks[b][u1 / 5][(u1 % 5) * 4]) = make_uint2(kreg.z, kreg.w);
    }
    if (ldv) *reinterpret_cast<uint4*>(&Vs[b][vrow][vcol]) = vreg;
  };
  f32x16 o = f32x16{};
  float m = -1e30f;
  gload(0); sstore(0);
  __syncthreads();
  const int NT = S_ / KVB;
  for (int t = 0; t < NT; ++t) {
    const int b = t & 1;
    if (t + 1 < NT) gload(t + 1);
    f32x16 s0 = f32x16{}, s1 = f32x16{};
    {
      bf16x8 k00 = *reinterpret_cast<const bf16x8*>(&Ks[b][r32][hf * 8]);
      bf16x8 k01 = *reinterpret_cast<const bf16x8*>(&Ks[b][r32][16 + hf * 8]);
      bf16x8 k10 = *reinterpret_cast<const bf16x8*>(&Ks[b][32 + r32][hf * 8]);
      bf16x8 k11 = *reinterpret_cast<const bf16x8*>(&Ks[b][32 + r32][16 + hf * 8]);
      s0 = __builtin_amdgcn_mfma_f32_32x32x16_bf16(k00, qf0, s0, 0, 0, 0);
      s1 = __builtin_amdgcn_mfma_f32_32x32x16_bf16(k10, qf0, s1, 0, 0, 0);
      s0 = __builtin_amdgcn_mfma_f32_32x32x16_bf16(k01, qf1, s0, 0, 0, 0);
      s1 = __builtin_amdgcn_mfma_f32_32x32x16_bf16(k11, qf1, s1, 0, 0, 0);
    }
    float mx = s0[0];
#pragma unroll
    for (int r = 1; r < 16; ++r) mx = fmaxf(mx, s0[r]);
#pragma unroll
    for (int r = 0; r < 16; ++r) mx = fmaxf(mx, s1[r]);
    {
      auto rr = __builtin_amdgcn_permlane32_swap(__float_as_uint(mx), __float_as_uint(mx), false, false);
      mx = fmaxf(__uint_as_float(rr[0]), __uint_as_float(rr[1]));
    }
    const float mn = fmaxf(m, mx);
    const float alpha = __builtin_amdgcn_exp2f(m - mn);
    m = mn;
#pragma unroll
    for (int r = 0; r < 16; ++r) { s0[r] = __builtin_amdgcn_exp2f(s0[r] - mn); s1[r] = __builtin_amdgcn_exp2f(s1[r] - mn); }
#pragma unroll
    for (int r = 0; r < 16; ++r) o[r] *= alpha;
    bf16x8 p00, p01, p10, p11;
    {
      uint4 w;
      w = make_uint4(cvtpk(s0[0], s0[1]), cvtpk(s0[2], s0[3]), cvtpk(s0[4], s0[5]), cvtpk(s0[6], s0[7])); p00 = *reinterpret_cast<bf16x8*>(&w);
      w = make_uint4(cvtpk(s0[8], s0[9]), cvtpk(s0[10], s0[11]), cvtpk(s0[12], s0[13]), cvtpk(s0[14], s0[15])); p01 = *reinterpret_cast<bf16x8*>(&w);
      w = make_uint4(cvtpk(s1[0], s1[1]), cvtpk(s1[2], s1[3]), cvtpk(s1[4], s1[5]), cvtpk(s1[6], s1[7])); p10 = *reinterpret_cast<bf16x8*>(&w);
      w = make_uint4(cvtpk(s1[8], s1[9]), cvtpk(s1[10], s1[11]), cvtpk(s1[12], s1[13]), cvtpk(s1[14], s1[15])); p11 = *reinterpret_cast<bf16x8*>(&w);
    }
    {
      bf16x8 v00 = *reinterpret_cast<const bf16x8*>(&Vs[b][r32][hf * 8]);
      bf16x8 v01 = *reinterpret_cast<const bf16x8*>(&Vs[b][r32][16 + hf * 8]);
      bf16x8 v10 = *reinterpret_cast<const bf16x8*>(&Vs[b][r32][32 + hf * 8]);
      bf16x8 v11 = *reinterpret_cast<const bf16x8*>(&Vs[b][r32][48 + hf * 8]);
      o = __builtin_amdgcn_mfma_f32_32x32x16_bf16(v00, p00, o, 0, 0, 0);
      o = __builtin_amdgcn_mfma_f32_32x32x16_bf16(v01, p01, o, 0, 0, 0);
      o = __builtin_amdgcn_mfma_f32_32x32x16_bf16(v10, p10, o, 0, 0, 0);
      o = __builtin_amdgcn_mfma_f32_32x32x16_bf16(v11, p11, o, 0, 0, 0);
    }
    if (t + 1 < NT) sstore(b ^ 1);
    __syncthreads();
  }
  const float l = __shfl(o[8], r32 + 32);
  const float inv = 1.f / l;
  u16* op = O + (size_t)q * D_ + h * DH;
#pragma unroll
  for (int g = 0; g < 3; ++g) {
    const int d = 8 * g + 4 * hf;
    if (d < DH) *reinterpret_cast<uint2*>(op + d) = pack4(o[4 * g] * inv, o[4 * g + 1] * inv, o[4 * g + 2] * inv, o[4 * g + 3] * inv);
  }
}

__global__ __launch_bounds__(256) void out_kernel(const u16* __restrict__ O, const u16* __restrict__ wo, u16* __restrict__ out) {
  constexpr int BM = 64, BN = 64;
  __shared__ GemmSmem<BM, BN> sm;
  const int m0 = blockIdx.y * BM, n0 = blockIdx.x * BN;
  f32x16 acc[1][1];
  gemm_mainloop<BM, BN, 1, 1>(O, D_, wo, D_, m0, n0, D_, sm, acc, true);
  const int tid = threadIdx.x, lane = tid & 63, wid = tid >> 6;
  const int wm = wid >> 1, wn = wid & 1, r32 = lane & 31, hf = lane >> 5;
  const int s = m0 + wm * 32 + r32;
#pragma unroll
  for (int g = 0; g < 4; ++g) {
    const int n = n0 + wn * 32 + 8 * g + 4 * hf;
    *reinterpret_cast<uint2*>(out + (size_t)s * D_ + n) = pack4(acc[0][0][4 * g], acc[0][0][4 * g + 1], acc[0][0][4 * g + 2], acc[0][0][4 * g + 3]);
  }
}

extern "C" void kernel_launch(void* const* d_in, const int* in_sizes, int n_in,
                              void* d_out, int out_size, void* d_ws, size_t ws_size,
                              hipStream_t stream) {
  const u16* x = (const u16*)d_in[0];
  const u16* wqkv = (const u16*)d_in[1];
  const u16* wo = (const u16*)d_in[2];
  u16* out = (u16*)d_out;
  char* ws = (char*)d_ws;
  const size_t headsz = (size_t)H_ * S_ * DH * 2;
  u16* Qb = (u16*)(ws);
  u16* Kb = (u16*)(ws + headsz);
  u16* Vt = (u16*)(ws + 2 * headsz);
  u16* Ob = (u16*)(ws + 3 * headsz);
  qkv_kernel<<<dim3(N3 / 128, S_ / 128), 256, 0, stream>>>(x, wqkv, Qb, Kb, Vt);
  attn_kernel<<<dim3(S_ / 128, H_), 256, 0, stream>>>(Qb, Kb, Vt, Ob);
  out_kernel<<<dim3(D_ / 64, S_ / 64), 256, 0, stream>>>(Ob, wo, out);
}
```

```cpp
#include <hip/hip_runtime.h>
#include <hip/hip_bf16.h>
#include <stdint.h>


typedef unsigned short u16;
typedef __attribute__((ext_vector_type(8))) short bf16x8;
typedef __attribute__((ext_vector_type(4))) short s16x4;
typedef __attribute__((ext_vector_type(16))) float f32x16;
typedef __attribute__((ext_vector_type(4))) float f32x4;
typedef __attribute__((ext_vector_type(4))) unsigned u32x4;

constexpr int S_ = 2048, D_ = 640, H_ = 32, DH = 20, N3 = 1920;
constexpr int BK = 64;

__device__ __forceinline__ unsigned cvtpk(float lo, float hi) {
  unsigned r; asm("v_cvt_pk_bf16_f32 %0, %1, %2" : "=v"(r) : "v"(lo), "v"(hi)); return r;
}
__device__ __forceinline__ uint2 pack4(float a, float b, float c, float d) {
  uint2 r; r.x = cvtpk(a, b); r.y = cvtpk(c, d); return r;
}
__device__ __forceinline__ s16x4 tr_read(const u16* p) {
  typedef __attribute__((address_space(3))) s16x4* lp;
  return __builtin_amdgcn_ds_read_tr16_b64_v4i16((lp)p);
}

template <int BM, int BN>
struct GemmSmem {
  u16 As[2][BM][BK + 8];
  u16 Bs[2][BK][BN + 32];
};

template <int BM, int BN, int TM, int TN>
__device__ __forceinline__ void gemm_mainloop(const u16* __restrict__ A, int lda, const u16* __restrict__ B, int ldb,
                                              int m0, int n0, int K, GemmSmem<BM, BN>& sm, f32x16 (&acc)[TM][TN], bool swap) {
  const int tid = threadIdx.x, lane = tid & 63, wid = tid >> 6;
  const int wm = wid >> 1, wn = wid & 1;
  const int r32 = lane & 31, hf = lane >> 5;
  constexpr int NA = BM * BK / 8 / 256, NB = BK * BN / 8 / 256;
  u32x4 ra[NA], rb[NB];
#define GEMM_GLOAD(kt)                                                                         \
  _Pragma("unroll") for (int i = 0; i < NA; ++i) {                                             \
    int c = tid + i * 256; int row = c >> 3, col = (c & 7) * 8;                                \
    ra[i] = *reinterpret_cast<const u32x4*>(A + (size_t)(m0 + row) * lda + (kt) * BK + col);   \
  }                                                                                            \
  _Pragma("unroll") for (int i = 0; i < NB; ++i) {                                             \
    int c = tid + i * 256; int row = c / (BN / 8), col = (c % (BN / 8)) * 8;                   \
    rb[i] = *reinterpret_cast<const u32x4*>(B + (size_t)((kt) * BK + row) * ldb + n0 + col);   \
  }
#define GEMM_SSTORE(bb)                                                                        \
  _Pragma("unroll") for (int i = 0; i < NA; ++i) {                                             \
    int c = tid + i * 256; int row = c >> 3, col = (c & 7) * 8;                                \
    *reinterpret_cast<u32x4*>(&sm.As[bb][row][col]) = ra[i];                                       \
  }                                                                                            \
  _Pragma("unroll") for (int i = 0; i < NB; ++i) {                                             \
    int c = tid + i * 256; int row = c / (BN / 8), col = (c % (BN / 8)) * 8;                   \
    *reinterpret_cast<u32x4*>(&sm.Bs[bb][row][col]) = rb[i];                                       \
  }
#pragma unroll
  for (int i = 0; i < TM; ++i)
#pragma unroll
    for (int j = 0; j < TN; ++j) acc[i][j] = f32x16{};
  const int KT = K / BK;
  GEMM_GLOAD(0);
  GEMM_SSTORE(0);
  GEMM_GLOAD(1);
  for (int kt = 0; kt < KT; ++kt) {
    const int cb = kt & 1;
    __syncthreads();
    if (kt + 1 < KT) { GEMM_SSTORE(cb ^ 1); }
    if (kt + 2 < KT) { GEMM_GLOAD(kt + 2); }
#pragma unroll
    for (int ks = 0; ks < BK / 16; ++ks) {
      bf16x8 af[TM], bfr[TN];
#pragma unroll
      for (int i = 0; i < TM; ++i)
        af[i] = *reinterpret_cast<const bf16x8*>(&sm.As[cb][wm * TM * 32 + i * 32 + r32][ks * 16 + hf * 8]);
#pragma unroll
      for (int j = 0; j < TN; ++j) {
        const int t = lane & 15, g = (lane >> 4) & 1;
        const u16* p = &sm.Bs[cb][ks * 16 + hf * 8 + (t >> 2)][wn * TN * 32 + j * 32 + g * 16 + (t & 3) * 4];
        s16x4 v0 = tr_read(p), v1 = tr_read(p + 4 * (BN + 32));
        bfr[j] = bf16x8{v0[0], v0[1], v0[2], v0[3], v1[0], v1[1], v1[2], v1[3]};
      }
      if (!swap) {
#pragma unroll
        for (int i = 0; i < TM; ++i)
#pragma unroll
          for (int j = 0; j < TN; ++j) acc[i][j] = __builtin_amdgcn_mfma_f32_32x32x16_bf16(af[i], bfr[j], acc[i][j], 0, 0, 0);
      } else {
#pragma unroll
        for (int i = 0; i < TM; ++i)
#pragma unroll
          for (int j = 0; j < TN; ++j) acc[i][j] = __builtin_amdgcn_mfma_f32_32x32x16_bf16(bfr[j], af[i], acc[i][j], 0, 0, 0);
      }
    }
  }
}

__global__ __launch_bounds__(256) void qkv_kernel(const u16* __restrict__ x, const u16* __restrict__ w,
                                                  u16* __restrict__ Qb, u16* __restrict__ Kb, u16* __restrict__ Vt) {
  constexpr int BM = 64, BN = 128;
  __shared__ GemmSmem<BM, BN> sm;
  const int m0 = blockIdx.y * BM, n0 = blockIdx.x * BN;
  const bool isv = n0 >= 2 * D_;
  f32x16 acc[1][2];
  gemm_mainloop<BM, BN, 1, 2>(x, D_, w, N3, m0, n0, D_, sm, acc, !isv);
  const int tid = threadIdx.x, lane = tid & 63, wid = tid >> 6;
  const int wm = wid >> 1, wn = wid & 1, r32 = lane & 31, hf = lane >> 5;
  const float qscale = 0.22360679774997896f * 1.4426950408889634f;
  if (!isv) {
#pragma unroll
    for (int i = 0; i < 1; ++i)
#pragma unroll
      for (int j = 0; j < 2; ++j) {
        const int s = m0 + wm * 32 + i * 32 + r32;
#pragma unroll
        for (int g = 0; g < 4; ++g) {
          const int n = n0 + wn * 64 + j * 32 + 8 * g + 4 * hf;
          const int which = n >= D_ ? 1 : 0;
          const int nn = n - which * D_;
          const int h = nn / DH, d = nn - h * DH;
          const float sc = which ? 1.f : qscale;
          uint2 v = pack4(acc[i][j][4 * g] * sc, acc[i][j][4 * g + 1] * sc, acc[i][j][4 * g + 2] * sc, acc[i][j][4 * g + 3] * sc);
          u16* dst = (which ? Kb : Qb) + ((size_t)h * S_ + s) * DH + d;
          *reinterpret_cast<uint2*>(dst) = v;
        }
      }
  } else {
#pragma unroll
    for (int i = 0; i < 1; ++i)
#pragma unroll
      for (int j = 0; j < 2; ++j) {
        const int nn = n0 + wn * 64 + j * 32 + r32 - 2 * D_;
        const int h = nn / DH, d = nn - h * DH;
#pragma unroll
        for (int g = 0; g < 4; ++g) {
          const int s = m0 + wm * 32 + i * 32 + 8 * g + 4 * hf;
          const int pos = (s & ~0xC) | ((s & 4) << 1) | ((s & 8) >> 1);
          uint2 v = pack4(acc[i][j][4 * g], acc[i][j][4 * g + 1], acc[i][j][4 * g + 2], acc[i][j][4 * g + 3]);
          *reinterpret_cast<uint2*>(Vt + ((size_t)h * DH + d) * S_ + pos) = v;
        }
      }
  }
}

constexpr int KSTR = 40;
constexpr int VSTR = 72;
constexpr int KVB = 64;

__global__ __launch_bounds__(256, 2) void attn_kernel(const u16* __restrict__ Qb, const u16* __restrict__ Kb,
                                                   const u16* __restrict__ Vt, u16* __restrict__ O) {
  __shared__ u16 Ks[2][KVB][KSTR];
  __shared__ u16 Vs[2][32][VSTR];
  const int tid = threadIdx.x, lane = tid & 63, wid = tid >> 6;
  const int r32 = lane & 31, hf = lane >> 5;
  const int h = blockIdx.y;
  const int q = blockIdx.x * 128 + wid * 32 + r32;
  for (int i = tid; i < 2 * KVB * (KSTR - DH); i += 256) {
    int b = i / (KVB * (KSTR - DH)), rem = i % (KVB * (KSTR - DH));
    Ks[b][rem / (KSTR - DH)][DH + rem % (KSTR - DH)] = (rem % (KSTR - DH) == 0) ? (u16)0x3F80 : (u16)0;
  }
  for (int i = tid; i < 2 * 12 * VSTR; i += 256) {
    int b = i / (12 * VSTR), rem = i % (12 * VSTR);
    int row = DH + rem / VSTR;
    Vs[b][row][rem % VSTR] = (row == DH) ? (u16)0x3F80 : (u16)0;
  }
  bf16x8 qf0, qf1;
  {
    const u16* qp = Qb + ((size_t)h * S_ + q) * DH;
    uint2 a0 = *reinterpret_cast<const uint2*>(qp + hf * 8);
    uint2 a1 = *reinterpret_cast<const uint2*>(qp + hf * 8 + 4);
    uint2 b0 = *reinterpret_cast<const uint2*>(qp + 16);
    uint4 f0 = make_uint4(a0.x, a0.y, a1.x, a1.y);
    uint4 f1 = hf ? make_uint4(0, 0, 0, 0) : make_uint4(b0.x, b0.y, 0, 0);
    qf0 = *reinterpret_cast<bf16x8*>(&f0); qf1 = *reinterpret_cast<bf16x8*>(&f1);
  }
  const u16* Kh = Kb + (size_t)h * S_ * DH;
  const u16* Vh = Vt + (size_t)h * DH * S_;
  u32x4 kreg, vreg;
  const bool ldk = tid < 160, ldv = tid >= 96;
  const int vc = tid - 96, vrow = vc >> 3, vcol = (vc & 7) * 8;
  const int ku0 = 2 * tid, ku1 = 2 * tid + 1;
#define K_GLOAD(t) if (ldk) kreg = *reinterpret_cast<const u32x4*>(Kh + (size_t)(t) * KVB * DH + tid * 8);
#define V_GLOAD(t) if (ldv) vreg = *reinterpret_cast<const u32x4*>(Vh + (size_t)vrow * S_ + (t) * KVB + vcol);
#define K_SSTORE(b) if (ldk) { \
      *reinterpret_cast<uint2*>(&Ks[b][ku0 / 5][(ku0 % 5) * 4]) = make_uint2(kreg[0], kreg[1]); \
      *reinterpret_cast<uint2*>(&Ks[b][ku1 / 5][(ku1 % 5) * 4]) = make_uint2(kreg[2], kreg[3]); }
#define V_SSTORE(b) if (ldv) *reinterpret_cast<u32x4*>(&Vs[b][vrow][vcol]) = vreg;
#define QK(b, S0, S1) { \
      bf16x8 k00 = *reinterpret_cast<const bf16x8*>(&Ks[b][r32][hf * 8]); \
      bf16x8 k01 = *reinterpret_cast<const bf16x8*>(&Ks[b][r32][16 + hf * 8]); \
      bf16x8 k10 = *reinterpret_cast<const bf16x8*>(&Ks[b][32 + r32][hf * 8]); \
      bf16x8 k11 = *reinterpret_cast<const bf16x8*>(&Ks[b][32 + r32][16 + hf * 8]); \
      S0 = __builtin_amdgcn_mfma_f32_32x32x16_bf16(k00, qf0, f32x16{}, 0, 0, 0); \
      S1 = __builtin_amdgcn_mfma_f32_32x32x16_bf16(k10, qf0, f32x16{}, 0, 0, 0); \
      S0 = __builtin_amdgcn_mfma_f32_32x32x16_bf16(k01, qf1, S0, 0, 0, 0); \
      S1 = __builtin_amdgcn_mfma_f32_32x32x16_bf16(k11, qf1, S1, 0, 0, 0); }
  constexpr float THR = 6.0f;
  f32x16 o = f32x16{};
  float mref = 0.f;
  f32x16 sa0, sa1, sb0, sb1;
  const int NT = S_ / KVB;
  K_GLOAD(0); V_GLOAD(0); K_SSTORE(0); V_SSTORE(0);
  K_GLOAD(1); K_SSTORE(1);
  __syncthreads();
  QK(0, sa0, sa1);
#define STEP(t, b, S0, S1, N0, N1) { \
    if ((t) + 2 < NT) { K_GLOAD((t) + 2); } \
    if ((t) + 1 < NT) { V_GLOAD((t) + 1); } \
    float mx = S0[0]; \
    _Pragma("unroll") for (int r = 1; r < 16; ++r) mx = fmaxf(mx, S0[r]); \
    _Pragma("unroll") for (int r = 0; r < 16; ++r) mx = fmaxf(mx, S1[r]); \
    { auto rr = __builtin_amdgcn_permlane32_swap(__float_as_uint(mx), __float_as_uint(mx), false, false); \
      mx = fmaxf(__uint_as_float(rr[0]), __uint_as_float(rr[1])) - mref; } \
    if ((t) == 0 || __any(mx > THR)) { \
      const float mnew = mref + (((t) == 0) ? mx : fmaxf(mx, 0.f)); \
      const unsigned nb = cvtpk(-mnew, 0.f) & 0xFFFFu; \
      const float mq = -__uint_as_float(nb << 16); \
      const float delta = mq - mref; \
      mref = mq; \
      const float alpha = __builtin_amdgcn_exp2f(-delta); \
      _Pragma("unroll") for (int r = 0; r < 16; ++r) { o[r] *= alpha; } \
    } \
    if ((t) + 1 < NT) QK((b) ^ 1, N0, N1); \
    _Pragma("unroll") for (int r = 0; r < 16; ++r) { S0[r] = __builtin_amdgcn_exp2f(S0[r] - mref); S1[r] = __builtin_amdgcn_exp2f(S1[r] - mref); } \
    bf16x8 p00, p01, p10, p11; \
    { u32x4 w; \
      w = u32x4{cvtpk(S0[0], S0[1]), cvtpk(S0[2], S0[3]), cvtpk(S0[4], S0[5]), cvtpk(S0[6], S0[7])}; p00 = *reinterpret_cast<bf16x8*>(&w); \
      w = u32x4{cvtpk(S0[8], S0[9]), cvtpk(S0[10], S0[11]), cvtpk(S0[12], S0[13]), cvtpk(S0[14], S0[15])}; p01 = *reinterpret_cast<bf16x8*>(&w); \
      w = u32x4{cvtpk(S1[0], S1[1]), cvtpk(S1[2], S1[3]), cvtpk(S1[4], S1[5]), cvtpk(S1[6], S1[7])}; p10 = *reinterpret_cast<bf16x8*>(&w); \
      w = u32x4{cvtpk(S1[8], S1[9]), cvtpk(S1[10], S1[11]), cvtpk(S1[12], S1[13]), cvtpk(S1[14], S1[15])}; p11 = *reinterpret_cast<bf16x8*>(&w); } \
    { bf16x8 v00 = *reinterpret_cast<const bf16x8*>(&Vs[b][r32][hf * 8]); \
      bf16x8 v01 = *reinterpret_cast<const bf16x8*>(&Vs[b][r32][16 + hf * 8]); \
      bf16x8 v10 = *reinterpret_cast<const bf16x8*>(&Vs[b][r32][32 + hf * 8]); \
      bf16x8 v11 = *reinterpret_cast<const bf16x8*>(&Vs[b][r32][48 + hf * 8]); \
      o = __builtin_amdgcn_mfma_f32_32x32x16_bf16(v00, p00, o, 0, 0, 0); \
      o = __builtin_amdgcn_mfma_f32_32x32x16_bf16(v01, p01, o, 0, 0, 0); \
      o = __builtin_amdgcn_mfma_f32_32x32x16_bf16(v10, p10, o, 0, 0, 0); \
      o = __builtin_amdgcn_mfma_f32_32x32x16_bf16(v11, p11, o, 0, 0, 0); } \
    if ((t) + 2 < NT) { K_SSTORE(b); } \
    if ((t) + 1 < NT) { V_SSTORE((b) ^ 1); } \
    __syncthreads(); }
  for (int t = 0; t < NT; t += 2) {
    STEP(t, 0, sa0, sa1, sb0, sb1);
    STEP(t + 1, 1, sb0, sb1, sa0, sa1);
  }
  const float l = __shfl(o[8], r32 + 32);
  const float inv = 1.f / l;
  u16* op = O + (size_t)q * D_ + h * DH;
#pragma unroll
  for (int g = 0; g < 3; ++g) {
    const int d = 8 * g + 4 * hf;
    if (d < DH) *reinterpret_cast<uint2*>(op + d) = pack4(o[4 * g] * inv, o[4 * g + 1] * inv, o[4 * g + 2] * inv, o[4 * g + 3] * inv);
  }
}

__global__ __launch_bounds__(256) void out_kernel(const u16* __restrict__ O, const u16* __restrict__ wo, u16* __restrict__ out) {
  constexpr int BM = 64, BN = 64;
  __shared__ GemmSmem<BM, BN> sm;
  const int m0 = blockIdx.y * BM, n0 = blockIdx.x * BN;
  f32x16 acc[1][1];
  gemm_mainloop<BM, BN, 1, 1>(O, D_, wo, D_, m0, n0, D_, sm, acc, true);
  const int tid = threadIdx.x, lane = tid & 63, wid = tid >> 6;
  const int wm = wid >> 1, wn = wid & 1, r32 = lane & 31, hf = lane >> 5;
  const int s = m0 + wm * 32 + r32;
#pragma unroll
  for (int g = 0; g < 4; ++g) {
    const int n = n0 + wn * 32 + 8 * g + 4 * hf;
    *reinterpret_cast<uint2*>(out + (size_t)s * D_ + n) = pack4(acc[0][0][4 * g], acc[0][0][4 * g + 1], acc[0][0][4 * g + 2], acc[0][0][4 * g + 3]);
  }
}

extern "C" void kernel_launch(void* const* d_in, const int* in_sizes, int n_in,
                              void* d_out, int out_size, void* d_ws, size_t ws_size,
                              hipStream_t stream) {
  const u16* x = (const u16*)d_in[0];
  const u16* wqkv = (const u16*)d_in[1];
  const u16* wo = (const u16*)d_in[2];
  u16* out = (u16*)d_out;
  char* ws = (char*)d_ws;
  const size_t headsz = (size_t)H_ * S_ * DH * 2;
  u16* Qb = (u16*)(ws);
  u16* Kb = (u16*)(ws + headsz);
  u16* Vt = (u16*)(ws + 2 * headsz);
  u16* Ob = (u16*)(ws + 3 * headsz);
  qkv_kernel<<<dim3(N3 / 128, S_ / 64), 256, 0, stream>>>(x, wqkv, Qb, Kb, Vt);
  attn_kernel<<<dim3(S_ / 128, H_), 256, 0, stream>>>(Qb, Kb, Vt, Ob);
  out_kernel<<<dim3(D_ / 64, S_ / 64), 256, 0, stream>>>(Ob, wo, out);
}
```

```cpp
#include <hip/hip_runtime.h>
#include <hip/hip_bf16.h>
#include <stdint.h>


typedef unsigned short u16;
typedef __attribute__((ext_vector_type(8))) short bf16x8;
typedef __attribute__((ext_vector_type(4))) short s16x4;
typedef __attribute__((ext_vector_type(16))) float f32x16;
typedef __attribute__((ext_vector_type(4))) float f32x4;
typedef __attribute__((ext_vector_type(4))) unsigned u32x4;

constexpr int S_ = 2048, D_ = 640, H_ = 32, DH = 20, N3 = 1920;
constexpr int BK = 64;

typedef __attribute__((ext_vector_type(2))) float f32x2;
typedef __attribute__((ext_vector_type(2))) __bf16 bf16v2;
__device__ __forceinline__ unsigned cvtpk(float lo, float hi) {
  bf16v2 r = __builtin_convertvector(f32x2{lo, hi}, bf16v2);
  return __builtin_bit_cast(unsigned, r);
}
__device__ __forceinline__ uint2 pack4(float a, float b, float c, float d) {
  uint2 r; r.x = cvtpk(a, b); r.y = cvtpk(c, d); return r;
}
__device__ __forceinline__ s16x4 tr_read(const u16* p) {
  typedef __attribute__((address_space(3))) s16x4* lp;
  return __builtin_amdgcn_ds_read_tr16_b64_v4i16((lp)p);
}

template <int BM, int BN>
struct GemmSmem {
  u16 As[2][BM][BK + 8];
  u16 Bs[2][BK][BN + 32];
};

template <int BM, int BN, int TM, int TN>
__device__ __forceinline__ void gemm_mainloop(const u16* __restrict__ A, int lda, const u16* __restrict__ B, int ldb,
                                              int m0, int n0, int K, GemmSmem<BM, BN>& sm, f32x16 (&acc)[TM][TN], bool swap) {
  const int tid = threadIdx.x, lane = tid & 63, wid = tid >> 6;
  const int wm = wid >> 1, wn = wid & 1;
  const int r32 = lane & 31, hf = lane >> 5;
  constexpr int NA = BM * BK / 8 / 256, NB = BK * BN / 8 / 256;
  u32x4 ra[NA], rb[NB];
#define GEMM_GLOAD(kt)                                                                         \
  _Pragma("unroll") for (int i = 0; i < NA; ++i) {                                             \
    int c = tid + i * 256; int row = c >> 3, col = (c & 7) * 8;                                \
    ra[i] = *reinterpret_cast<const u32x4*>(A + (size_t)(m0 + row) * lda + (kt) * BK + col);   \
  }                                                                                            \
  _Pragma("unroll") for (int i = 0; i < NB; ++i) {                                             \
    int c = tid + i * 256; int row = c / (BN / 8), col = (c % (BN / 8)) * 8;                   \
    rb[i] = *reinterpret_cast<const u32x4*>(B + (size_t)((kt) * BK + row) * ldb + n0 + col);   \
  }
#define GEMM_SSTORE(bb)                                                                        \
  _Pragma("unroll") for (int i = 0; i < NA; ++i) {                                             \
    int c = tid + i * 256; int row = c >> 3, col = (c & 7) * 8;                                \
    *reinterpret_cast<u32x4*>(&sm.As[bb][row][col]) = ra[i];                                       \
  }                                                                                            \
  _Pragma("unroll") for (int i = 0; i < NB; ++i) {                                             \
    int c = tid + i * 256; int row = c / (BN / 8), col = (c % (BN / 8)) * 8;                   \
    *reinterpret_cast<u32x4*>(&sm.Bs[bb][row][col]) = rb[i];                                       \
  }
#pragma unroll
  for (int i = 0; i < TM; ++i)
#pragma unroll
    for (int j = 0; j < TN; ++j) acc[i][j] = f32x16{};
  const int KT = K / BK;
  GEMM_GLOAD(0);
  GEMM_SSTORE(0);
  GEMM_GLOAD(1);
  for (int kt = 0; kt < KT; ++kt) {
    const int cb = kt & 1;
    __syncthreads();
    if (kt + 1 < KT) { GEMM_SSTORE(cb ^ 1); }
    if (kt + 2 < KT) { GEMM_GLOAD(kt + 2); }
#pragma unroll
    for (int ks = 0; ks < BK / 16; ++ks) {
      bf16x8 af[TM], bfr[TN];
#pragma unroll
      for (int i = 0; i < TM; ++i)
        af[i] = *reinterpret_cast<const bf16x8*>(&sm.As[cb][wm * TM * 32 + i * 32 + r32][ks * 16 + hf * 8]);
#pragma unroll
      for (int j = 0; j < TN; ++j) {
        const int t = lane & 15, g = (lane >> 4) & 1;
        const u16* p = &sm.Bs[cb][ks * 16 + hf * 8 + (t >> 2)][wn * TN * 32 + j * 32 + g * 16 + (t & 3) * 4];
        s16x4 v0 = tr_read(p), v1 = tr_read(p + 4 * (BN + 32));
        bfr[j] = bf16x8{v0[0], v0[1], v0[2], v0[3], v1[0], v1[1], v1[2], v1[3]};
      }
      if (!swap) {
#pragma unroll
        for (int i = 0; i < TM; ++i)
#pragma unroll
          for (int j = 0; j < TN; ++j) acc[i][j] = __builtin_amdgcn_mfma_f32_32x32x16_bf16(af[i], bfr[j], acc[i][j], 0, 0, 0);
      } else {
#pragma unroll
        for (int i = 0; i < TM; ++i)
#pragma unroll
          for (int j = 0; j < TN; ++j) acc[i][j] = __builtin_amdgcn_mfma_f32_32x32x16_bf16(bfr[j], af[i], acc[i][j], 0, 0, 0);
      }
    }
  }
}

__global__ __launch_bounds__(256) void qkv_kernel(const u16* __restrict__ x, const u16* __restrict__ w,
                                                  u16* __restrict__ Qb, u16* __restrict__ Kb, u16* __restrict__ Vt) {
  constexpr int BM = 64, BN = 128;
  __shared__ GemmSmem<BM, BN> sm;
  const int m0 = blockIdx.y * BM, n0 = blockIdx.x * BN;
  const bool isv = n0 >= 2 * D_;
  f32x16 acc[1][2];
  gemm_mainloop<BM, BN, 1, 2>(x, D_, w, N3, m0, n0, D_, sm, acc, !isv);
  const int tid = threadIdx.x, lane = tid & 63, wid = tid >> 6;
  const int wm = wid >> 1, wn = wid & 1, r32 = lane & 31, hf = lane >> 5;
  const float qscale = 0.22360679774997896f * 1.4426950408889634f;
  if (!isv) {
#pragma unroll
    for (int i = 0; i < 1; ++i)
#pragma unroll
      for (int j = 0; j < 2; ++j) {
        const int s = m0 + wm * 32 + i * 32 + r32;
#pragma unroll
        for (int g = 0; g < 4; ++g) {
          const int n = n0 + wn * 64 + j * 32 + 8 * g + 4 * hf;
          const int which = n >= D_ ? 1 : 0;
          const int nn = n - which * D_;
          const int h = nn / DH, d = nn - h * DH;
          const float sc = which ? 1.f : qscale;
          uint2 v = pack4(acc[i][j][4 * g] * sc, acc[i][j][4 * g + 1] * sc, acc[i][j][4 * g + 2] * sc, acc[i][j][4 * g + 3] * sc);
          u16* dst = (which ? Kb : Qb) + ((size_t)h * S_ + s) * DH + d;
          *reinterpret_cast<uint2*>(dst) = v;
        }
      }
  } else {
#pragma unroll
    for (int i = 0; i < 1; ++i)
#pragma unroll
      for (int j = 0; j < 2; ++j) {
        const int nn = n0 + wn * 64 + j * 32 + r32 - 2 * D_;
        const int h = nn / DH, d = nn - h * DH;
#pragma unroll
        for (int g = 0; g < 4; ++g) {
          const int s = m0 + wm * 32 + i * 32 + 8 * g + 4 * hf;
          const int pos = (s & ~0xC) | ((s & 4) << 1) | ((s & 8) >> 1);
          uint2 v = pack4(acc[i][j][4 * g], acc[i][j][4 * g + 1], acc[i][j][4 * g + 2], acc[i][j][4 * g + 3]);
          *reinterpret_cast<uint2*>(Vt + ((size_t)h * DH + d) * S_ + pos) = v;
        }
      }
  }
}

constexpr int KSTR = 40;
constexpr int VSTR = 72;
constexpr int KVB = 64;

__global__ __launch_bounds__(512) __attribute__((amdgpu_waves_per_eu(4, 4)))
void attn_kernel(const u16* __restrict__ Qb, const u16* __restrict__ Kb, const u16* __restrict__ Vt, u16* __restrict__ O) {
  __shared__ __attribute__((aligned(16))) u16 Ks[4][2][KVB][KSTR];
  __shared__ __attribute__((aligned(16))) u16 Vs[2][2][32][VSTR];
  __shared__ int badflag;
  const int tid = threadIdx.x, lane = tid & 63, wid = tid >> 6;
  const int r32 = lane & 31, hf = lane >> 5;
  const int qg = wid & 3, kh = wid >> 2;
  const int bid = blockIdx.x;
  const int slot = bid >> 3;
  const int h = (bid & 7) * 4 + (slot >> 4);
  const int q = (slot & 15) * 128 + qg * 32 + r32;
  {
    uint2* kp = reinterpret_cast<uint2*>(&Ks[0][0][0][0] + tid * KSTR + DH);
    kp[0] = make_uint2(0x3F80u, 0u); kp[1] = make_uint2(0u, 0u); kp[2] = make_uint2(0u, 0u); kp[3] = make_uint2(0u, 0u); kp[4] = make_uint2(0u, 0u);
    if (tid < 432) {
      const int vt = tid / 108, c = tid % 108;
      const unsigned val = (c < 9) ? 0x3F803F80u : 0u;
      *reinterpret_cast<u32x4*>(&Vs[0][0][0][0] + vt * 32 * VSTR + DH * VSTR + c * 8) = u32x4{val, val, val, val};
    }
  }
  if (tid == 0) badflag = 0;
  bf16x8 qf0; u32x4 qf1;
  {
    const u16* qp = Qb + ((size_t)h * S_ + q) * DH;
    uint2 a0 = *reinterpret_cast<const uint2*>(qp + hf * 8);
    uint2 a1 = *reinterpret_cast<const uint2*>(qp + hf * 8 + 4);
    uint2 b0 = *reinterpret_cast<const uint2*>(qp + 16);
    qf0 = __builtin_bit_cast(bf16x8, u32x4{a0.x, a0.y, a1.x, a1.y});
    qf1 = hf ? u32x4{0, 0, 0, 0} : u32x4{b0.x, b0.y, 0, 0};
  }
  const u16* Kh = Kb + (size_t)h * S_ * DH;
  const u16* Vh = Vt + (size_t)h * DH * S_;
  u32x4 kreg, vreg;
  const bool ldk = tid < 320, ldv = tid >= 192;
  const int kc = tid % 160, khalf = tid / 160;
  const int vc0 = tid - 192, vhalf = vc0 / 160, vc = vc0 % 160, vrow = vc >> 3, vcol = (vc & 7) * 8;
  const int ku0 = 2 * kc, ku1 = 2 * kc + 1;
  const u16* kgp = Kh + (size_t)(khalf * 1024) * DH + kc * 8;
  const u16* vgp = Vh + (size_t)vrow * S_ + vhalf * 1024 + vcol;
#define K_GLOAD(t) if (ldk) kreg = *reinterpret_cast<const u32x4*>(kgp + (size_t)(t) * KVB * DH);
#define V_GLOAD(t) if (ldv) vreg = *reinterpret_cast<const u32x4*>(vgp + (t) * KVB);
#define K_SSTORE(b) if (ldk) { \
      *reinterpret_cast<uint2*>(&Ks[b][khalf][ku0 / 5][(ku0 % 5) * 4]) = make_uint2(kreg[0], kreg[1]); \
      *reinterpret_cast<uint2*>(&Ks[b][khalf][ku1 / 5][(ku1 % 5) * 4]) = make_uint2(kreg[2], kreg[3]); }
#define V_SSTORE(b) if (ldv) *reinterpret_cast<u32x4*>(&Vs[b][vhalf][vrow][vcol]) = vreg;
#define QKS(b, sub, S0) { \
      bf16x8 k0 = *reinterpret_cast<const bf16x8*>(&Ks[b][kh][(sub) * 32 + r32][hf * 8]); \
      bf16x8 k1 = *reinterpret_cast<const bf16x8*>(&Ks[b][kh][(sub) * 32 + r32][16 + hf * 8]); \
      S0 = __builtin_amdgcn_mfma_f32_32x32x16_bf16(k0, qf0, f32x16{}, 0, 0, 0); \
      S0 = __builtin_amdgcn_mfma_f32_32x32x16_bf16(k1, __builtin_bit_cast(bf16x8, qf1), S0, 0, 0, 0); }
#define PVS(b, sub, S0) { \
      bf16x8 p0 = __builtin_bit_cast(bf16x8, u32x4{cvtpk(S0[0], S0[1]), cvtpk(S0[2], S0[3]), cvtpk(S0[4], S0[5]), cvtpk(S0[6], S0[7])}); \
      bf16x8 p1 = __builtin_bit_cast(bf16x8, u32x4{cvtpk(S0[8], S0[9]), cvtpk(S0[10], S0[11]), cvtpk(S0[12], S0[13]), cvtpk(S0[14], S0[15])}); \
      bf16x8 v0 = *reinterpret_cast<const bf16x8*>(&Vs[b][kh][r32][(sub) * 32 + hf * 8]); \
      bf16x8 v1 = *reinterpret_cast<const bf16x8*>(&Vs[b][kh][r32][(sub) * 32 + 16 + hf * 8]); \
      o = __builtin_amdgcn_mfma_f32_32x32x16_bf16(v0, p0, o, 0, 0, 0); \
      o = __builtin_amdgcn_mfma_f32_32x32x16_bf16(v1, p1, o, 0, 0, 0); }
#define EXPS(S0) _Pragma("unroll") for (int r = 0; r < 16; ++r) S0[r] = __builtin_amdgcn_exp2f(S0[r]);
#define WAVEMAX(S0, S1, mx) { \
      mx = S0[0]; \
      _Pragma("unroll") for (int r = 1; r < 16; ++r) mx = fmaxf(mx, S0[r]); \
      _Pragma("unroll") for (int r = 0; r < 16; ++r) mx = fmaxf(mx, S1[r]); \
      auto rr = __builtin_amdgcn_permlane32_swap(__float_as_uint(mx), __float_as_uint(mx), false, false); \
      mx = fmaxf(__uint_as_float(rr[0]), __uint_as_float(rr[1])); }
  f32x16 o = f32x16{};
  float mref;
  f32x16 sa, sb;
  constexpr int NT = S_ / 2 / KVB;
  K_GLOAD(0); V_GLOAD(0); K_SSTORE(0); V_SSTORE(0);
  K_GLOAD(1); K_SSTORE(1);
  __syncthreads();
  {
    QKS(0, 0, sa); QKS(0, 1, sb);
    float mx; WAVEMAX(sa, sb, mx);
    const unsigned nb = cvtpk(-mx, 0.f) & 0xFFFFu;
    mref = -__uint_as_float(nb << 16);
    if (!hf) qf1[2] = nb;
  }
  QKS(0, 0, sa);
#define STEP(t, kb) { \
    if ((t) + 2 < NT) { K_GLOAD((t) + 2); } \
    if ((t) + 1 < NT) { V_GLOAD((t) + 1); } \
    QKS(kb, 1, sb); \
    EXPS(sa); PVS((kb) & 1, 0, sa); \
    if ((t) + 1 < NT) QKS(((kb) + 1) & 3, 0, sa); \
    EXPS(sb); PVS((kb) & 1, 1, sb); \
    if ((t) + 2 < NT) { K_SSTORE(((kb) + 2) & 3); } \
    if ((t) + 1 < NT) { V_SSTORE(((kb) + 1) & 1); } \
    __syncthreads(); }
  for (int t = 0; t < NT; t += 4) {
    STEP(t, 0); STEP(t + 1, 1); STEP(t + 2, 2); STEP(t + 3, 3);
  }
  if (hf && !(o[8] < 1.0e30f)) badflag = 1;
  __syncthreads();
  if (badflag) {
    o = f32x16{}; mref = -1.0e30f;
    if (!hf) qf1[2] = 0;
    for (int t = 0; t < NT; ++t) {
      K_GLOAD(t); V_GLOAD(t);
      __syncthreads();
      K_SSTORE(0); V_SSTORE(0);
      __syncthreads();
      QKS(0, 0, sa); QKS(0, 1, sb);
      float mx; WAVEMAX(sa, sb, mx);
      const float mnew = fmaxf(mref, mx);
      const float alpha = __builtin_amdgcn_exp2f(mref - mnew);
      mref = mnew;
#pragma unroll
      for (int r = 0; r < 16; ++r) { o[r] *= alpha; sa[r] -= mref; sb[r] -= mref; }
      EXPS(sa); EXPS(sb);
      PVS(0, 0, sa); PVS(0, 1, sb);
    }
    __syncthreads();
  }
  float* mb = reinterpret_cast<float*>(&Ks[0][0][0][0]) + (qg * 17) * 64 + lane;
  if (kh == 1) {
#pragma unroll
    for (int r = 0; r < 16; ++r) mb[r * 64] = o[r];
    mb[16 * 64] = mref;
  }
  __syncthreads();
  if (kh == 0) {
    const float m1 = mb[16 * 64];
    const float mm = fmaxf(mref, m1);
    const float a0 = __builtin_amdgcn_exp2f(mref - mm), a1 = __builtin_amdgcn_exp2f(m1 - mm);
#pragma unroll
    for (int r = 0; r < 16; ++r) o[r] = o[r] * a0 + mb[r * 64] * a1;
    const float l = __shfl(o[8], r32 + 32);
    const float inv = 1.f / l;
    u16* op = O + (size_t)q * D_ + h * DH;
#pragma unroll
    for (int g = 0; g < 3; ++g) {
      const int d = 8 * g + 4 * hf;
      if (d < DH) *reinterpret_cast<uint2*>(op + d) = pack4(o[4 * g] * inv, o[4 * g + 1] * inv, o[4 * g + 2] * inv, o[4 * g + 3] * inv);
    }
  }
}

__global__ __launch_bounds__(256) void out_kernel(const u16* __restrict__ O, const u16* __restrict__ wo, u16* __restrict__ out) {
  constexpr int BM = 64, BN = 64;
  __shared__ GemmSmem<BM, BN> sm;
  const int m0 = blockIdx.y * BM, n0 = blockIdx.x * BN;
  f32x16 acc[1][1];
  gemm_mainloop<BM, BN, 1, 1>(O, D_, wo, D_, m0, n0, D_, sm, acc, true);
  const int tid = threadIdx.x, lane = tid & 63, wid = tid >> 6;
  const int wm = wid >> 1, wn = wid & 1, r32 = lane & 31, hf = lane >> 5;
  const int s = m0 + wm * 32 + r32;
#pragma unroll
  for (int g = 0; g < 4; ++g) {
    const int n = n0 + wn * 32 + 8 * g + 4 * hf;
    *reinterpret_cast<uint2*>(out + (size_t)s * D_ + n) = pack4(acc[0][0][4 * g], acc[0][0][4 * g + 1], acc[0][0][4 * g + 2], acc[0][0][4 * g + 3]);
  }
}

extern "C" void kernel_launch(void* const* d_in, const int* in_sizes, int n_in,
                              void* d_out, int out_size, void* d_ws, size_t ws_size,
                              hipStream_t stream) {
  const u16* x = (const u16*)d_in[0];
  const u16* wqkv = (const u16*)d_in[1];
  const u16* wo = (const u16*)d_in[2];
  u16* out = (u16*)d_out;
  char* ws = (char*)d_ws;
  const size_t headsz = (size_t)H_ * S_ * DH * 2;
  u16* Qb = (u16*)(ws);
  u16* Kb = (u16*)(ws + headsz);
  u16* Vt = (u16*)(ws + 2 * headsz);
  u16* Ob = (u16*)(ws + 3 * headsz);
  qkv_kernel<<<dim3(N3 / 128, S_ / 64), 256, 0, stream>>>(x, wqkv, Qb, Kb, Vt);
  attn_kernel<<<dim3(S_ / 128 * H_), 512, 0, stream>>>(Qb, Kb, Vt, Ob);
  out_kernel<<<dim3(D_ / 64, S_ / 64), 256, 0, stream>>>(Ob, wo, out);
}
```

```cpp
#include <hip/hip_runtime.h>
#include <hip/hip_bf16.h>
#include <stdint.h>

typedef unsigned short u16;
typedef __attribute__((ext_vector_type(8))) short bf16x8;
typedef __attribute__((ext_vector_type(4))) short s16x4;
typedef __attribute__((ext_vector_type(16))) float f32x16;
typedef __attribute__((ext_vector_type(4))) float f32x4;
typedef __attribute__((ext_vector_type(4))) unsigned u32x4;

constexpr int S_ = 2048, D_ = 640, H_ = 32, DH = 20, N3 = 1920;
constexpr int BK = 64;
#define QBM 128
#define QPF 2
#define OPF 4

typedef __attribute__((ext_vector_type(2))) float f32x2;
typedef __attribute__((ext_vector_type(2))) __bf16 bf16v2;
__device__ __forceinline__ unsigned cvtpk(float lo, float hi) {
  bf16v2 r = __builtin_convertvector(f32x2{lo, hi}, bf16v2);
  return __builtin_bit_cast(unsigned, r);
}
__device__ __forceinline__ uint2 pack4(float a, float b, float c, float d) {
  uint2 r; r.x = cvtpk(a, b); r.y = cvtpk(c, d); return r;
}
__device__ __forceinline__ s16x4 tr_read(const u16* p) {
  typedef __attribute__((address_space(3))) s16x4* lp;
  return __builtin_amdgcn_ds_read_tr16_b64_v4i16((lp)p);
}

template <int BM, int BN>
struct GemmSmem {
  u16 As[2][BM][BK + 8];
  u16 Bs[2][BK][BN + 32];
};

template <int BM, int BN, int TM, int TN, bool swap, int KT, int PF>
__device__ __forceinline__ void gemm_mainloop(const u16* __restrict__ A, int lda, const u16* __restrict__ B, int ldb,
                                              int m0, int n0, GemmSmem<BM, BN>& sm, f32x16 (&acc)[TM][TN]) {
  const int tid = threadIdx.x, lane = tid & 63, wid = tid >> 6;
  const int wm = wid >> 1, wn = wid & 1;
  const int r32 = lane & 31, hf = lane >> 5;
  constexpr int NA = BM * BK / 8 / 256, NB = BK * BN / 8 / 256;
  u32x4 ra[PF][NA], rb[PF][NB];
  const u16* ap = A + (size_t)(m0 + (tid >> 3)) * lda + (tid & 7) * 8;
  const u16* bp = B + (size_t)(tid / (BN / 8)) * ldb + n0 + (tid % (BN / 8)) * 8;
#define GEMM_GLOAD(kt, st)                                                                     \
  _Pragma("unroll") for (int i = 0; i < NA; ++i)                                               \
    ra[st][i] = *reinterpret_cast<const u32x4*>(ap + (size_t)(i * 32) * lda + (kt) * BK);      \
  _Pragma("unroll") for (int i = 0; i < NB; ++i)                                               \
    rb[st][i] = *reinterpret_cast<const u32x4*>(bp + (size_t)((kt) * BK + i * (256 / (BN / 8))) * ldb);
#define GEMM_SSTORE(bb, st)                                                                    \
  _Pragma("unroll") for (int i = 0; i < NA; ++i)                                               \
    *reinterpret_cast<u32x4*>(&sm.As[bb][(tid >> 3) + i * 32][(tid & 7) * 8]) = ra[st][i];     \
  _Pragma("unroll") for (int i = 0; i < NB; ++i)                                               \
    *reinterpret_cast<u32x4*>(&sm.Bs[bb][tid / (BN / 8) + i * (256 / (BN / 8))][(tid % (BN / 8)) * 8]) = rb[st][i];
#pragma unroll
  for (int i = 0; i < TM; ++i)
#pragma unroll
    for (int j = 0; j < TN; ++j) acc[i][j] = f32x16{};
#pragma unroll
  for (int p = 0; p < PF; ++p) { GEMM_GLOAD(p, p); }
  GEMM_SSTORE(0, 0);
  if (PF < KT) { GEMM_GLOAD(PF, 0); }
#pragma unroll
  for (int kt = 0; kt < KT; ++kt) {
    const int cb = kt & 1;
    __syncthreads();
    if (kt + 1 < KT) { GEMM_SSTORE(cb ^ 1, (kt + 1) % PF); }
    if (kt + 1 + PF < KT) { GEMM_GLOAD(kt + 1 + PF, (kt + 1) % PF); }
#pragma unroll
    for (int ks = 0; ks < BK / 16; ++ks) {
      bf16x8 af[TM], bfr[TN];
#pragma unroll
      for (int i = 0; i < TM; ++i)
        af[i] = *reinterpret_cast<const bf16x8*>(&sm.As[cb][wm * TM * 32 + i * 32 + r32][ks * 16 + hf * 8]);
#pragma unroll
      for (int j = 0; j < TN; ++j) {
        const int t = lane & 15, g = (lane >> 4) & 1;
        const u16* p = &sm.Bs[cb][ks * 16 + hf * 8 + (t >> 2)][wn * TN * 32 + j * 32 + g * 16 + (t & 3) * 4];
        s16x4 v0 = tr_read(p), v1 = tr_read(p + 4 * (BN + 32));
        bfr[j] = bf16x8{v0[0], v0[1], v0[2], v0[3], v1[0], v1[1], v1[2], v1[3]};
      }
#pragma unroll
      for (int i = 0; i < TM; ++i)
#pragma unroll
        for (int j = 0; j < TN; ++j)
          acc[i][j] = swap ? __builtin_amdgcn_mfma_f32_32x32x16_bf16(bfr[j], af[i], acc[i][j], 0, 0, 0)
                           : __builtin_amdgcn_mfma_f32_32x32x16_bf16(af[i], bfr[j], acc[i][j], 0, 0, 0);
    }
  }
}

__global__ __launch_bounds__(256) void qkv_kernel(const u16* __restrict__ x, const u16* __restrict__ w,
                                                  u16* __restrict__ Qb, u16* __restrict__ Kb, u16* __restrict__ Vt) {
  constexpr int BM = QBM, BN = 128, TM = QBM / 64;
  __shared__ GemmSmem<BM, BN> sm;
  const int m0 = blockIdx.y * BM, n0 = blockIdx.x * BN;
  const bool isv = n0 >= 2 * D_;
  f32x16 acc[TM][2];
  const int tid = threadIdx.x, lane = tid & 63, wid = tid >> 6;
  const int wm = wid >> 1, wn = wid & 1, r32 = lane & 31, hf = lane >> 5;
  const float qscale = 0.22360679774997896f * 1.4426950408889634f;
  if (!isv) {
    gemm_mainloop<BM, BN, TM, 2, true, D_ / BK, QPF>(x, D_, w, N3, m0, n0, sm, acc);
#pragma unroll
    for (int i = 0; i < TM; ++i)
#pragma unroll
      for (int j = 0; j < 2; ++j) {
        const int s = m0 + wm * TM * 32 + i * 32 + r32;
#pragma unroll
        for (int g = 0; g < 4; ++g) {
          const int n = n0 + wn * 64 + j * 32 + 8 * g + 4 * hf;
          const int which = n >= D_ ? 1 : 0;
          const int nn = n - which * D_;
          const int h = nn / DH, d = nn - h * DH;
          const float sc = which ? 1.f : qscale;
          uint2 v = pack4(acc[i][j][4 * g] * sc, acc[i][j][4 * g + 1] * sc, acc[i][j][4 * g + 2] * sc, acc[i][j][4 * g + 3] * sc);
          u16* dst = (which ? Kb : Qb) + ((size_t)h * S_ + s) * DH + d;
          *reinterpret_cast<uint2*>(dst) = v;
        }
      }
  } else {
    gemm_mainloop<BM, BN, TM, 2, false, D_ / BK, QPF>(x, D_, w, N3, m0, n0, sm, acc);
#pragma unroll
    for (int i = 0; i < TM; ++i)
#pragma unroll
      for (int j = 0; j < 2; ++j) {
        const int nn = n0 + wn * 64 + j * 32 + r32 - 2 * D_;
        const int h = nn / DH, d = nn - h * DH;
#pragma unroll
        for (int g = 0; g < 4; ++g) {
          const int s = m0 + wm * TM * 32 + i * 32 + 8 * g + 4 * hf;
          const int pos = (s & ~0xC) | ((s & 4) << 1) | ((s & 8) >> 1);
          uint2 v = pack4(acc[i][j][4 * g], acc[i][j][4 * g + 1], acc[i][j][4 * g + 2], acc[i][j][4 * g + 3]);
          *reinterpret_cast<uint2*>(Vt + ((size_t)h * DH + d) * S_ + pos) = v;
        }
      }
  }
}

constexpr int KSTR = 40;
constexpr int VSTR = 72;
constexpr int KVB = 64;

__global__ __launch_bounds__(512) __attribute__((amdgpu_waves_per_eu(4, 4)))
void attn_kernel(const u16* __restrict__ Qb, const u16* __restrict__ Kb, const u16* __restrict__ Vt, u16* __restrict__ O) {
  __shared__ __attribute__((aligned(16))) u16 Ks[4][2][KVB][KSTR];
  __shared__ __attribute__((aligned(16))) u16 Vs[2][2][32][VSTR];
  __shared__ int badflag;
  const int tid = threadIdx.x, lane = tid & 63, wid = tid >> 6;
  const int r32 = lane & 31, hf = lane >> 5;
  const int qg = wid & 3, kh = wid >> 2;
  const int bid = blockIdx.x;
  const int slot = bid >> 3;
  const int h = (bid & 7) * 4 + (slot >> 4);
  const int q = (slot & 15) * 128 + qg * 32 + r32;
  {
    uint2* kp = reinterpret_cast<uint2*>(&Ks[0][0][0][0] + tid * KSTR + DH);
    kp[0] = make_uint2(0x3F80u, 0u); kp[1] = make_uint2(0u, 0u); kp[2] = make_uint2(0u, 0u); kp[3] = make_uint2(0u, 0u); kp[4] = make_uint2(0u, 0u);
    if (tid < 432) {
      const int vt = tid / 108, c = tid % 108;
      const unsigned val = (c < 9) ? 0x3F803F80u : 0u;
      *reinterpret_cast<u32x4*>(&Vs[0][0][0][0] + vt * 32 * VSTR + DH * VSTR + c * 8) = u32x4{val, val, val, val};
    }
  }
  if (tid == 0) badflag = 0;
  bf16x8 qf0; u32x4 qf1;
  {
    const u16* qp = Qb + ((size_t)h * S_ + q) * DH;
    uint2 a0 = *reinterpret_cast<const uint2*>(qp + hf * 8);
    uint2 a1 = *reinterpret_cast<const uint2*>(qp + hf * 8 + 4);
    uint2 b0 = *reinterpret_cast<const uint2*>(qp + 16);
    qf0 = __builtin_bit_cast(bf16x8, u32x4{a0.x, a0.y, a1.x, a1.y});
    qf1 = hf ? u32x4{0, 0, 0, 0} : u32x4{b0.x, b0.y, 0, 0};
  }
  const u16* Kh = Kb + (size_t)h * S_ * DH;
  const u16* Vh = Vt + (size_t)h * DH * S_;
  u32x4 kreg, vreg;
  const bool ldk = tid < 320, ldv = tid >= 192;
  const int kc = tid % 160, khalf = tid / 160;
  const int vc0 = tid - 192, vhalf = vc0 / 160, vc = vc0 % 160, vrow = vc >> 3, vcol = (vc & 7) * 8;
  const int ku0 = 2 * kc, ku1 = 2 * kc + 1;
  const u16* kgp = Kh + (size_t)(khalf * 1024) * DH + kc * 8;
  const u16* vgp = Vh + (size_t)vrow * S_ + vhalf * 1024 + vcol;
#define K_GLOAD(t) if (ldk) kreg = *reinterpret_cast<const u32x4*>(kgp + (size_t)(t) * KVB * DH);
#define V_GLOAD(t) if (ldv) vreg = *reinterpret_cast<const u32x4*>(vgp + (t) * KVB);
#define K_SSTORE(b) if (ldk) { \
      *reinterpret_cast<uint2*>(&Ks[b][khalf][ku0 / 5][(ku0 % 5) * 4]) = make_uint2(kreg[0], kreg[1]); \
      *reinterpret_cast<uint2*>(&Ks[b][khalf][ku1 / 5][(ku1 % 5) * 4]) = make_uint2(kreg[2], kreg[3]); }
#define V_SSTORE(b) if (ldv) *reinterpret_cast<u32x4*>(&Vs[b][vhalf][vrow][vcol]) = vreg;
#define QKS(b, sub, S0) { \
      bf16x8 k0 = *reinterpret_cast<const bf16x8*>(&Ks[b][kh][(sub) * 32 + r32][hf * 8]); \
      bf16x8 k1 = *reinterpret_cast<const bf16x8*>(&Ks[b][kh][(sub) * 32 + r32][16 + hf * 8]); \
      S0 = __builtin_amdgcn_mfma_f32_32x32x16_bf16(k0, qf0, f32x16{}, 0, 0, 0); \
      S0 = __builtin_amdgcn_mfma_f32_32x32x16_bf16(k1, __builtin_bit_cast(bf16x8, qf1), S0, 0, 0, 0); }
#define PVS(b, sub, S0) { \
      bf16x8 p0 = __builtin_bit_cast(bf16x8, u32x4{cvtpk(S0[0], S0[1]), cvtpk(S0[2], S0[3]), cvtpk(S0[4], S0[5]), cvtpk(S0[6], S0[7])}); \
      bf16x8 p1 = __builtin_bit_cast(bf16x8, u32x4{cvtpk(S0[8], S0[9]), cvtpk(S0[10], S0[11]), cvtpk(S0[12], S0[13]), cvtpk(S0[14], S0[15])}); \
      bf16x8 v0 = *reinterpret_cast<const bf16x8*>(&Vs[b][kh][r32][(sub) * 32 + hf * 8]); \
      bf16x8 v1 = *reinterpret_cast<const bf16x8*>(&Vs[b][kh][r32][(sub) * 32 + 16 + hf * 8]); \
      o = __builtin_amdgcn_mfma_f32_32x32x16_bf16(v0, p0, o, 0, 0, 0); \
      o = __builtin_amdgcn_mfma_f32_32x32x16_bf16(v1, p1, o, 0, 0, 0); }
#define EXPS(S0) _Pragma("unroll") for (int r = 0; r < 16; ++r) S0[r] = __builtin_amdgcn_exp2f(S0[r]);
#define WAVEMAX(S0, S1, mx) { \
      mx = S0[0]; \
      _Pragma("unroll") for (int r = 1; r < 16; ++r) mx = fmaxf(mx, S0[r]); \
      _Pragma("unroll") for (int r = 0; r < 16; ++r) mx = fmaxf(mx, S1[r]); \
      auto rr = __builtin_amdgcn_permlane32_swap(__float_as_uint(mx), __float_as_uint(mx), false, false); \
      mx = fmaxf(__uint_as_float(rr[0]), __uint_as_float(rr[1])); }
  f32x16 o = f32x16{};
  float mref;
  f32x16 sa, sb;
  constexpr int NT = S_ / 2 / KVB;
  K_GLOAD(0); V_GLOAD(0); K_SSTORE(0); V_SSTORE(0);
  K_GLOAD(1); K_SSTORE(1);
  __syncthreads();
  {
    QKS(0, 0, sa); QKS(0, 1, sb);
    float mx; WAVEMAX(sa, sb, mx);
    const unsigned nb = cvtpk(-mx, 0.f) & 0xFFFFu;
    mref = -__uint_as_float(nb << 16);
    if (!hf) qf1[2] = nb;
  }
  QKS(0, 0, sa);
#define STEP(t, kb) { \
    if ((t) + 2 < NT) { K_GLOAD((t) + 2); } \
    if ((t) + 1 < NT) { V_GLOAD((t) + 1); } \
    QKS(kb, 1, sb); \
    EXPS(sa); PVS((kb) & 1, 0, sa); \
    if ((t) + 1 < NT) QKS(((kb) + 1) & 3, 0, sa); \
    EXPS(sb); PVS((kb) & 1, 1, sb); \
    if ((t) + 2 < NT) { K_SSTORE(((kb) + 2) & 3); } \
    if ((t) + 1 < NT) { V_SSTORE(((kb) + 1) & 1); } \
    __syncthreads(); }
  for (int t = 0; t < NT; t += 4) {
    STEP(t, 0); STEP(t + 1, 1); STEP(t + 2, 2); STEP(t + 3, 3);
  }
  if (hf && !(o[8] < 1.0e30f)) badflag = 1;
  __syncthreads();
  if (badflag) {
    o = f32x16{}; mref = -1.0e30f;
    if (!hf) qf1[2] = 0;
    for (int t = 0; t < NT; ++t) {
      K_GLOAD(t); V_GLOAD(t);
      __syncthreads();
      K_SSTORE(0); V_SSTORE(0);
      __syncthreads();
      QKS(0, 0, sa); QKS(0, 1, sb);
      float mx; WAVEMAX(sa, sb, mx);
      const float mnew = fmaxf(mref, mx);
      const float alpha = __builtin_amdgcn_exp2f(mref - mnew);
      mref = mnew;
#pragma unroll
      for (int r = 0; r < 16; ++r) { o[r] *= alpha; sa[r] -= mref; sb[r] -= mref; }
      EXPS(sa); EXPS(sb);
      PVS(0, 0, sa); PVS(0, 1, sb);
    }
    __syncthreads();
  }
  float* mb = reinterpret_cast<float*>(&Ks[0][0][0][0]) + (qg * 17) * 64 + lane;
  if (kh == 1) {
#pragma unroll
    for (int r = 0; r < 16; ++r) mb[r * 64] = o[r];
    mb[16 * 64] = mref;
  }
  __syncthreads();
  if (kh == 0) {
    const float m1 = mb[16 * 64];
    const float mm = fmaxf(mref, m1);
    const float a0 = __builtin_amdgcn_exp2f(mref - mm), a1 = __builtin_amdgcn_exp2f(m1 - mm);
#pragma unroll
    for (int r = 0; r < 16; ++r) o[r] = o[r] * a0 + mb[r * 64] * a1;
    const float l = __shfl(o[8], r32 + 32);
    const float inv = 1.f / l;
    u16* op = O + (size_t)q * D_ + h * DH;
#pragma unroll
    for (int g = 0; g < 3; ++g) {
      const int d = 8 * g + 4 * hf;
      if (d < DH) *reinterpret_cast<uint2*>(op + d) = pack4(o[4 * g] * inv, o[4 * g + 1] * inv, o[4 * g + 2] * inv, o[4 * g + 3] * inv);
    }
  }
}

__global__ __launch_bounds__(256) void out_kernel(const u16* __restrict__ O, const u16* __restrict__ wo, u16* __restrict__ out) {
  constexpr int BM = 64, BN = 64;
  __shared__ GemmSmem<BM, BN> sm;
  const int m0 = blockIdx.y * BM, n0 = blockIdx.x * BN;
  f32x16 acc[1][1];
  gemm_mainloop<BM, BN, 1, 1, true, D_ / BK, OPF>(O, D_, wo, D_, m0, n0, sm, acc);
  const int tid = threadIdx.x, lane = tid & 63, wid = tid >> 6;
  const int wm = wid >> 1, wn = wid & 1, r32 = lane & 31, hf = lane >> 5;
  const int s = m0 + wm * 32 + r32;
#pragma unroll
  for (int g = 0; g < 4; ++g) {
    const int n = n0 + wn * 32 + 8 * g + 4 * hf;
    *reinterpret_cast<uint2*>(out + (size_t)s * D_ + n) = pack4(acc[0][0][4 * g], acc[0][0][4 * g + 1], acc[0][0][4 * g + 2], acc[0][0][4 * g + 3]);
  }
}

extern "C" void kernel_launch(void* const* d_in, const int* in_sizes, int n_in,
                              void* d_out, int out_size, void* d_ws, size_t ws_size,
                              hipStream_t stream) {
  const u16* x = (const u16*)d_in[0];
  const u16* wqkv = (const u16*)d_in[1];
  const u16* wo = (const u16*)d_in[2];
  u16* out = (u16*)d_out;
  char* ws = (char*)d_ws;
  const size_t headsz = (size_t)H_ * S_ * DH * 2;
  u16* Qb = (u16*)(ws);
  u16* Kb = (u16*)(ws + headsz);
  u16* Vt = (u16*)(ws + 2 * headsz);
  u16* Ob = (u16*)(ws + 3 * headsz);
  qkv_kernel<<<dim3(N3 / 128, S_ / QBM), 256, 0, stream>>>(x, wqkv, Qb, Kb, Vt);
  attn_kernel<<<dim3(S_ / 128 * H_), 512, 0, stream>>>(Qb, Kb, Vt, Ob);
  out_kernel<<<dim3(D_ / 64, S_ / 64), 256, 0, stream>>>(Ob, wo, out);
}
```

```cpp
#include <hip/hip_runtime.h>
#include <hip/hip_bf16.h>
#include <stdint.h>

typedef unsigned short u16;
typedef __attribute__((ext_vector_type(8))) short bf16x8;
typedef __attribute__((ext_vector_type(4))) short s16x4;
typedef __attribute__((ext_vector_type(16))) float f32x16;
typedef __attribute__((ext_vector_type(4))) float f32x4;
typedef __attribute__((ext_vector_type(4))) unsigned u32x4;

constexpr int S_ = 2048, D_ = 640, H_ = 32, DH = 20, N3 = 1920;
constexpr int BK = 64;
#define QBM 128
#define QPF 2
#define OPF 4

typedef __attribute__((ext_vector_type(2))) float f32x2;
typedef __attribute__((ext_vector_type(2))) __bf16 bf16v2;
__device__ __forceinline__ unsigned cvtpk(float lo, float hi) {
  bf16v2 r = __builtin_convertvector(f32x2{lo, hi}, bf16v2);
  return __builtin_bit_cast(unsigned, r);
}
__device__ __forceinline__ uint2 pack4(float a, float b, float c, float d) {
  uint2 r; r.x = cvtpk(a, b); r.y = cvtpk(c, d); return r;
}
__device__ __forceinline__ s16x4 tr_read(const u16* p) {
  typedef __attribute__((address_space(3))) s16x4* lp;
  return __builtin_amdgcn_ds_read_tr16_b64_v4i16((lp)p);
}

template <int BM, int BN>
struct GemmSmem {
  u16 As[2][BM][BK + 8];
  u16 Bs[2][BK][BN + 32];
};

template <int BM, int BN, int TM, int TN, bool swap, int KT, int PF>
__device__ __forceinline__ void gemm_mainloop(const u16* __restrict__ A, int lda, const u16* __restrict__ B, int ldb,
                                              int m0, int n0, GemmSmem<BM, BN>& sm, f32x16 (&acc)[TM][TN]) {
  const int tid = threadIdx.x, lane = tid & 63, wid = tid >> 6;
  const int wm = wid >> 1, wn = wid & 1;
  const int r32 = lane & 31, hf = lane >> 5;
  constexpr int NA = BM * BK / 8 / 256, NB = BK * BN / 8 / 256;
  u32x4 ra[PF][NA], rb[PF][NB];
  const u16* ap = A + (size_t)(m0 + (tid >> 3)) * lda + (tid & 7) * 8;
  const u16* bp = B + (size_t)(tid / (BN / 8)) * ldb + n0 + (tid % (BN / 8)) * 8;
#define GEMM_GLOAD(kt, st)                                                                     \
  _Pragma("unroll") for (int i = 0; i < NA; ++i)                                               \
    ra[st][i] = *reinterpret_cast<const u32x4*>(ap + (size_t)(i * 32) * lda + (kt) * BK);      \
  _Pragma("unroll") for (int i = 0; i < NB; ++i)                                               \
    rb[st][i] = *reinterpret_cast<const u32x4*>(bp + (size_t)((kt) * BK + i * (256 / (BN / 8))) * ldb);
#define GEMM_SSTORE(bb, st)                                                                    \
  _Pragma("unroll") for (int i = 0; i < NA; ++i)                                               \
    *reinterpret_cast<u32x4*>(&sm.As[bb][(tid >> 3) + i * 32][(tid & 7) * 8]) = ra[st][i];     \
  _Pragma("unroll") for (int i = 0; i < NB; ++i)                                               \
    *reinterpret_cast<u32x4*>(&sm.Bs[bb][tid / (BN / 8) + i * (256 / (BN / 8))][(tid % (BN / 8)) * 8]) = rb[st][i];
#pragma unroll
  for (int i = 0; i < TM; ++i)
#pragma unroll
    for (int j = 0; j < TN; ++j) acc[i][j] = f32x16{};
#pragma unroll
  for (int p = 0; p < PF; ++p) { GEMM_GLOAD(p, p); }
  GEMM_SSTORE(0, 0);
  if (PF < KT) { GEMM_GLOAD(PF, 0); }
#pragma unroll
  for (int kt = 0; kt < KT; ++kt) {
    const int cb = kt & 1;
    __syncthreads();
    if (kt + 1 < KT) { GEMM_SSTORE(cb ^ 1, (kt + 1) % PF); }
    if (kt + 1 + PF < KT) { GEMM_GLOAD(kt + 1 + PF, (kt + 1) % PF); }
#pragma unroll
    for (int ks = 0; ks < BK / 16; ++ks) {
      bf16x8 af[TM], bfr[TN];
#pragma unroll
      for (int i = 0; i < TM; ++i)
        af[i] = *reinterpret_cast<const bf16x8*>(&sm.As[cb][wm * TM * 32 + i * 32 + r32][ks * 16 + hf * 8]);
#pragma unroll
      for (int j = 0; j < TN; ++j) {
        const int t = lane & 15, g = (lane >> 4) & 1;
        const u16* p = &sm.Bs[cb][ks * 16 + hf * 8 + (t >> 2)][wn * TN * 32 + j * 32 + g * 16 + (t & 3) * 4];
        s16x4 v0 = tr_read(p), v1 = tr_read(p + 4 * (BN + 32));
        bfr[j] = bf16x8{v0[0], v0[1], v0[2], v0[3], v1[0], v1[1], v1[2], v1[3]};
      }
#pragma unroll
      for (int i = 0; i < TM; ++i)
#pragma unroll
        for (int j = 0; j < TN; ++j)
          acc[i][j] = swap ? __builtin_amdgcn_mfma_f32_32x32x16_bf16(bfr[j], af[i], acc[i][j], 0, 0, 0)
                           : __builtin_amdgcn_mfma_f32_32x32x16_bf16(af[i], bfr[j], acc[i][j], 0, 0, 0);
    }
  }
}

template <int BM, int BN>
struct GemmSmem3 {
  u16 As[3][BM][BK + 8];
  u16 Bs[3][BK][BN + 32];
};

template <int BM, int BN, int TM, int TN, bool swap, int KT, int PF>
__device__ __forceinline__ void gemm3_mainloop(const u16* __restrict__ A, int lda, const u16* __restrict__ B, int ldb,
                                               int m0, int n0, GemmSmem3<BM, BN>& sm, f32x16 (&acc)[TM][TN]) {
  const int tid = threadIdx.x, lane = tid & 63, wid = tid >> 6;
  const int wm = wid >> 1, wn = wid & 1;
  const int r32 = lane & 31, hf = lane >> 5;
  constexpr int NA = BM * BK / 8 / 256, NB = BK * BN / 8 / 256;
  u32x4 ra[PF][NA], rb[PF][NB];
  const u16* ap = A + (size_t)(m0 + (tid >> 3)) * lda + (tid & 7) * 8;
  const u16* bp = B + (size_t)(tid / (BN / 8)) * ldb + n0 + (tid % (BN / 8)) * 8;
  bf16x8 af[2][TM], bfr[2][TN];
  const int t16 = lane & 15, g16 = (lane >> 4) & 1;
#define G3_FREAD(bb, ks, fs)                                                                                     \
  _Pragma("unroll") for (int i = 0; i < TM; ++i)                                                                 \
    af[fs][i] = *reinterpret_cast<const bf16x8*>(&sm.As[bb][wm * TM * 32 + i * 32 + r32][(ks) * 16 + hf * 8]);  \
  _Pragma("unroll") for (int j = 0; j < TN; ++j) {                                                               \
    const u16* p = &sm.Bs[bb][(ks) * 16 + hf * 8 + (t16 >> 2)][wn * TN * 32 + j * 32 + g16 * 16 + (t16 & 3) * 4]; \
    s16x4 v0 = tr_read(p), v1 = tr_read(p + 4 * (BN + 32));                                                      \
    bfr[fs][j] = bf16x8{v0[0], v0[1], v0[2], v0[3], v1[0], v1[1], v1[2], v1[3]};                                 \
  }
#pragma unroll
  for (int i = 0; i < TM; ++i)
#pragma unroll
    for (int j = 0; j < TN; ++j) acc[i][j] = f32x16{};
  GEMM_GLOAD(0, 0);
  GEMM_GLOAD(1, 1 % PF);
  if (PF == 1) { }
  GEMM_SSTORE(0, 0);
  if (PF == 1) { GEMM_GLOAD(1, 0); }
  GEMM_SSTORE(1, 1 % PF);
#pragma unroll
  for (int p = 0; p < PF; ++p) { GEMM_GLOAD(2 + p, (2 + p) % PF); }
  __syncthreads();
  G3_FREAD(0, 0, 0);
#pragma unroll
  for (int kt = 0; kt < KT; ++kt) {
    const int cb = kt % 3;
    if (kt > 0) __syncthreads();
#pragma unroll
    for (int ks = 0; ks < BK / 16; ++ks) {
      const int fs = ks & 1;
      if (ks + 1 < BK / 16) { G3_FREAD(cb, ks + 1, fs ^ 1); }
      else if (kt + 1 < KT) { G3_FREAD((kt + 1) % 3, 0, fs ^ 1); }
      if (ks == 0 && kt + 2 < KT) { GEMM_SSTORE((kt + 2) % 3, (kt + 2) % PF); }
      if (ks == 1 && kt + 2 + PF < KT) { GEMM_GLOAD(kt + 2 + PF, (kt + 2) % PF); }
#pragma unroll
      for (int i = 0; i < TM; ++i)
#pragma unroll
        for (int j = 0; j < TN; ++j)
          acc[i][j] = swap ? __builtin_amdgcn_mfma_f32_32x32x16_bf16(bfr[fs][j], af[fs][i], acc[i][j], 0, 0, 0)
                           : __builtin_amdgcn_mfma_f32_32x32x16_bf16(af[fs][i], bfr[fs][j], acc[i][j], 0, 0, 0);
    }
  }
}

__global__ __launch_bounds__(256) void qkv_kernel(const u16* __restrict__ x, const u16* __restrict__ w,
                                                  u16* __restrict__ Qb, u16* __restrict__ Kb, u16* __restrict__ Vt) {
  constexpr int BM = QBM, BN = 128, TM = QBM / 64;
  __shared__ GemmSmem3<BM, BN> sm;
  const int id = blockIdx.x, jj = id >> 3;
  const int m0 = ((id & 7) * 2 + jj / 15) * BM, n0 = (jj % 15) * BN;
  const bool isv = n0 >= 2 * D_;
  f32x16 acc[TM][2];
  const int tid = threadIdx.x, lane = tid & 63, wid = tid >> 6;
  const int wm = wid >> 1, wn = wid & 1, r32 = lane & 31, hf = lane >> 5;
  const float qscale = 0.22360679774997896f * 1.4426950408889634f;
  if (!isv) {
    gemm3_mainloop<BM, BN, TM, 2, true, D_ / BK, QPF>(x, D_, w, N3, m0, n0, sm, acc);
#pragma unroll
    for (int i = 0; i < TM; ++i)
#pragma unroll
      for (int j = 0; j < 2; ++j) {
        const int s = m0 + wm * TM * 32 + i * 32 + r32;
#pragma unroll
        for (int g = 0; g < 4; ++g) {
          const int n = n0 + wn * 64 + j * 32 + 8 * g + 4 * hf;
          const int which = n >= D_ ? 1 : 0;
          const float sc = which ? 1.f : qscale;
          uint2 v = pack4(acc[i][j][4 * g] * sc, acc[i][j][4 * g + 1] * sc, acc[i][j][4 * g + 2] * sc, acc[i][j][4 * g + 3] * sc);
          u16* dst = Qb + ((size_t)(n >> 2) * S_ + s) * 4;
          *reinterpret_cast<uint2*>(dst) = v;
        }
      }
  } else {
    gemm3_mainloop<BM, BN, TM, 2, false, D_ / BK, QPF>(x, D_, w, N3, m0, n0, sm, acc);
#pragma unroll
    for (int i = 0; i < TM; ++i)
#pragma unroll
      for (int j = 0; j < 2; ++j) {
        const int nn = n0 + wn * 64 + j * 32 + r32 - 2 * D_;
#pragma unroll
        for (int g = 0; g < 4; ++g) {
          const int s = m0 + wm * TM * 32 + i * 32 + 8 * g + 4 * hf;
          uint2 v = pack4(acc[i][j][4 * g], acc[i][j][4 * g + 1], acc[i][j][4 * g + 2], acc[i][j][4 * g + 3]);
          *reinterpret_cast<uint2*>(Vt + ((size_t)(s >> 2) * D_ + nn) * 4) = v;
        }
      }
  }
}

constexpr int KSTR = 40;
constexpr int VSTR = 72;
constexpr int KVB = 64;

__global__ __launch_bounds__(512) __attribute__((amdgpu_waves_per_eu(4, 4)))
void attn_kernel(const u16* __restrict__ Qb, const u16* __restrict__ Kb, const u16* __restrict__ Vt, u16* __restrict__ O) {
  __shared__ __attribute__((aligned(16))) u16 Ks[4][2][KVB][KSTR];
  __shared__ __attribute__((aligned(16))) u16 Vs[2][2][32][VSTR];
  __shared__ int badflag;
  const int tid = threadIdx.x, lane = tid & 63, wid = tid >> 6;
  const int r32 = lane & 31, hf = lane >> 5;
  const int qg = wid & 3, kh = wid >> 2;
  const int bid = blockIdx.x;
  const int slot = bid >> 3;
  const int h = (bid & 7) * 4 + (slot >> 4);
  const int q = (slot & 15) * 128 + qg * 32 + r32;
  {
    uint2* kp = reinterpret_cast<uint2*>(&Ks[0][0][0][0] + tid * KSTR + DH);
    kp[0] = make_uint2(0x3F80u, 0u); kp[1] = make_uint2(0u, 0u); kp[2] = make_uint2(0u, 0u); kp[3] = make_uint2(0u, 0u); kp[4] = make_uint2(0u, 0u);
    if (tid < 432) {
      const int vt = tid / 108, c = tid % 108;
      const unsigned val = (c < 9) ? 0x3F803F80u : 0u;
      *reinterpret_cast<u32x4*>(&Vs[0][0][0][0] + vt * 32 * VSTR + DH * VSTR + c * 8) = u32x4{val, val, val, val};
    }
  }
  if (tid == 0) badflag = 0;
  bf16x8 qf0; u32x4 qf1;
  {
    const u16* qp = Qb + ((size_t)(h * 5) * S_ + q) * 4;
    uint2 a0 = *reinterpret_cast<const uint2*>(qp + (size_t)(hf * 2) * S_ * 4);
    uint2 a1 = *reinterpret_cast<const uint2*>(qp + (size_t)(hf * 2 + 1) * S_ * 4);
    uint2 b0 = *reinterpret_cast<const uint2*>(qp + (size_t)4 * S_ * 4);
    qf0 = __builtin_bit_cast(bf16x8, u32x4{a0.x, a0.y, a1.x, a1.y});
    qf1 = hf ? u32x4{0, 0, 0, 0} : u32x4{b0.x, b0.y, 0, 0};
  }
  u32x4 kreg, vreg;
  const bool ldk = tid < 320, ldv = tid >= 192;
  const int kc = tid % 160, khalf = tid / 160, kq = kc >> 5, kj = kc & 31;
  const int vc0 = tid - 192, vhalf = vc0 / 160, vc = vc0 % 160, vsq = vc / 10, vdd = (vc % 10) * 2;
  const int vpos = ((vsq & ~3) | ((vsq & 1) << 1) | ((vsq & 2) >> 1)) * 4;
  const u16* kgp = Kb + ((size_t)(h * 5 + kq) * S_ + khalf * 1024 + 2 * kj) * 4;
  const u16* vgp = Vt + ((size_t)(vhalf * 256 + vsq) * D_ + h * DH + vdd) * 4;
#define K_GLOAD(t) if (ldk) kreg = *reinterpret_cast<const u32x4*>(kgp + (size_t)(t) * KVB * 4);
#define V_GLOAD(t) if (ldv) vreg = *reinterpret_cast<const u32x4*>(vgp + (size_t)(t) * 16 * D_ * 4);
#define K_SSTORE(b) if (ldk) { \
      *reinterpret_cast<uint2*>(&Ks[b][khalf][2 * kj][kq * 4]) = make_uint2(kreg[0], kreg[1]); \
      *reinterpret_cast<uint2*>(&Ks[b][khalf][2 * kj + 1][kq * 4]) = make_uint2(kreg[2], kreg[3]); }
#define V_SSTORE(b) if (ldv) { \
      *reinterpret_cast<uint2*>(&Vs[b][vhalf][vdd][vpos]) = make_uint2(vreg[0], vreg[1]); \
      *reinterpret_cast<uint2*>(&Vs[b][vhalf][vdd + 1][vpos]) = make_uint2(vreg[2], vreg[3]); }
#define QKS(b, sub, S0) { \
      bf16x8 k0 = *reinterpret_cast<const bf16x8*>(&Ks[b][kh][(sub) * 32 + r32][hf * 8]); \
      bf16x8 k1 = *reinterpret_cast<const bf16x8*>(&Ks[b][kh][(sub) * 32 + r32][16 + hf * 8]); \
      S0 = __builtin_amdgcn_mfma_f32_32x32x16_bf16(k0, qf0, f32x16{}, 0, 0, 0); \
      S0 = __builtin_amdgcn_mfma_f32_32x32x16_bf16(k1, __builtin_bit_cast(bf16x8, qf1), S0, 0, 0, 0); }
#define PVS(b, sub, S0) { \
      bf16x8 p0 = __builtin_bit_cast(bf16x8, u32x4{cvtpk(S0[0], S0[1]), cvtpk(S0[2], S0[3]), cvtpk(S0[4], S0[5]), cvtpk(S0[6], S0[7])}); \
      bf16x8 p1 = __builtin_bit_cast(bf16x8, u32x4{cvtpk(S0[8], S0[9]), cvtpk(S0[10], S0[11]), cvtpk(S0[12], S0[13]), cvtpk(S0[14], S0[15])}); \
      bf16x8 v0 = *reinterpret_cast<const bf16x8*>(&Vs[b][kh][r32][(sub) * 32 + hf * 8]); \
      bf16x8 v1 = *reinterpret_cast<const bf16x8*>(&Vs[b][kh][r32][(sub) * 32 + 16 + hf * 8]); \
      o = __builtin_amdgcn_mfma_f32_32x32x16_bf16(v0, p0, o, 0, 0, 0); \
      o = __builtin_amdgcn_mfma_f32_32x32x16_bf16(v1, p1, o, 0, 0, 0); }
#define EXPS(S0) _Pragma("unroll") for (int r = 0; r < 16; ++r) S0[r] = __builtin_amdgcn_exp2f(S0[r]);
#define WAVEMAX(S0, S1, mx) { \
      mx = S0[0]; \
      _Pragma("unroll") for (int r = 1; r < 16; ++r) mx = fmaxf(mx, S0[r]); \
      _Pragma("unroll") for (int r = 0; r < 16; ++r) mx = fmaxf(mx, S1[r]); \
      auto rr = __builtin_amdgcn_permlane32_swap(__float_as_uint(mx), __float_as_uint(mx), false, false); \
      mx = fmaxf(__uint_as_float(rr[0]), __uint_as_float(rr[1])); }
  f32x16 o = f32x16{};
  float mref;
  f32x16 sa, sb;
  constexpr int NT = S_ / 2 / KVB;
  K_GLOAD(0); V_GLOAD(0); K_SSTORE(0); V_SSTORE(0);
  K_GLOAD(1); K_SSTORE(1);
  __syncthreads();
  {
    QKS(0, 0, sa); QKS(0, 1, sb);
    float mx; WAVEMAX(sa, sb, mx);
    const unsigned nb = cvtpk(-mx, 0.f) & 0xFFFFu;
    mref = -__uint_as_float(nb << 16);
    if (!hf) qf1[2] = nb;
  }
  QKS(0, 0, sa);
#define STEP(t, kb) { \
    if ((t) + 2 < NT) { K_GLOAD((t) + 2); } \
    if ((t) + 1 < NT) { V_GLOAD((t) + 1); } \
    QKS(kb, 1, sb); \
    EXPS(sa); PVS((kb) & 1, 0, sa); \
    if ((t) + 1 < NT) QKS(((kb) + 1) & 3, 0, sa); \
    EXPS(sb); PVS((kb) & 1, 1, sb); \
    if ((t) + 2 < NT) { K_SSTORE(((kb) + 2) & 3); } \
    if ((t) + 1 < NT) { V_SSTORE(((kb) + 1) & 1); } \
    __syncthreads(); }
  for (int t = 0; t < NT; t += 4) {
    STEP(t, 0); STEP(t + 1, 1); STEP(t + 2, 2); STEP(t + 3, 3);
  }
  if (hf && !(o[8] < 1.0e30f)) badflag = 1;
  __syncthreads();
  if (badflag) {
    o = f32x16{}; mref = -1.0e30f;
    if (!hf) qf1[2] = 0;
    for (int t = 0; t < NT; ++t) {
      K_GLOAD(t); V_GLOAD(t);
      __syncthreads();
      K_SSTORE(0); V_SSTORE(0);
      __syncthreads();
      QKS(0, 0, sa); QKS(0, 1, sb);
      float mx; WAVEMAX(sa, sb, mx);
      const float mnew = fmaxf(mref, mx);
      const float alpha = __builtin_amdgcn_exp2f(mref - mnew);
      mref = mnew;
#pragma unroll
      for (int r = 0; r < 16; ++r) { o[r] *= alpha; sa[r] -= mref; sb[r] -= mref; }
      EXPS(sa); EXPS(sb);
      PVS(0, 0, sa); PVS(0, 1, sb);
    }
    __syncthreads();
  }
  float* mb = reinterpret_cast<float*>(&Ks[0][0][0][0]) + (qg * 17) * 64 + lane;
  if (kh == 1) {
#pragma unroll
    for (int r = 0; r < 16; ++r) mb[r * 64] = o[r];
    mb[16 * 64] = mref;
  }
  __syncthreads();
  if (kh == 0) {
    const float m1 = mb[16 * 64];
    const float mm = fmaxf(mref, m1);
    const float a0 = __builtin_amdgcn_exp2f(mref - mm), a1 = __builtin_amdgcn_exp2f(m1 - mm);
#pragma unroll
    for (int r = 0; r < 16; ++r) o[r] = o[r] * a0 + mb[r * 64] * a1;
    const float l = __shfl(o[8], r32 + 32);
    const float inv = 1.f / l;
    u16* op = O + (size_t)q * D_ + h * DH;
#pragma unroll
    for (int g = 0; g < 3; ++g) {
      const int d = 8 * g + 4 * hf;
      if (d < DH) *reinterpret_cast<uint2*>(op + d) = pack4(o[4 * g] * inv, o[4 * g + 1] * inv, o[4 * g + 2] * inv, o[4 * g + 3] * inv);
    }
  }
}

__global__ __launch_bounds__(256) void out_kernel(const u16* __restrict__ O, const u16* __restrict__ wo, u16* __restrict__ out) {
  constexpr int BM = 64, BN = 64;
  __shared__ GemmSmem<BM, BN> sm;
  const int id = blockIdx.x, jj = id >> 3;
  const int m0 = ((id & 7) * 4 + jj / 10) * BM, n0 = (jj % 10) * BN;
  f32x16 acc[1][1];
  gemm_mainloop<BM, BN, 1, 1, true, D_ / BK, OPF>(O, D_, wo, D_, m0, n0, sm, acc);
  const int tid = threadIdx.x, lane = tid & 63, wid = tid >> 6;
  const int wm = wid >> 1, wn = wid & 1, r32 = lane & 31, hf = lane >> 5;
  const int s = m0 + wm * 32 + r32;
#pragma unroll
  for (int g = 0; g < 4; ++g) {
    const int n = n0 + wn * 32 + 8 * g + 4 * hf;
    *reinterpret_cast<uint2*>(out + (size_t)s * D_ + n) = pack4(acc[0][0][4 * g], acc[0][0][4 * g + 1], acc[0][0][4 * g + 2], acc[0][0][4 * g + 3]);
  }
}

extern "C" void kernel_launch(void* const* d_in, const int* in_sizes, int n_in,
                              void* d_out, int out_size, void* d_ws, size_t ws_size,
                              hipStream_t stream) {
  const u16* x = (const u16*)d_in[0];
  const u16* wqkv = (const u16*)d_in[1];
  const u16* wo = (const u16*)d_in[2];
  u16* out = (u16*)d_out;
  char* ws = (char*)d_ws;
  const size_t headsz = (size_t)H_ * S_ * DH * 2;
  u16* Qb = (u16*)(ws);
  u16* Kb = (u16*)(ws + headsz);
  u16* Vt = (u16*)(ws + 2 * headsz);
  u16* Ob = (u16*)(ws + 3 * headsz);
  qkv_kernel<<<dim3(N3 / 128 * S_ / QBM), 256, 0, stream>>>(x, wqkv, Qb, Kb, Vt);
  attn_kernel<<<dim3(S_ / 128 * H_), 512, 0, stream>>>(Qb, Kb, Vt, Ob);
  out_kernel<<<dim3(D_ / 64 * S_ / 64), 256, 0, stream>>>(Ob, wo, out);
}
```
